```python
import torch

def _softmax(x, axis=-1):
    return torch.softmax(x, dim=axis)
def _gelu(x):
    return torch.nn.functional.gelu(x, approximate="tanh")
def _silu(x):
    return torch.nn.functional.silu(x)
def _sigmoid(x):
    return torch.sigmoid(x)
def _layernorm(x, axis=-1, eps=1e-5):
    m = x.mean(dim=axis, keepdim=True)
    v = x.var(dim=axis, keepdim=True, unbiased=False)
    return (x - m) / torch.sqrt(v + eps)
def _scatter_add(tgt, idx, src, axis=0):
    return tgt.clone().index_add_(axis, idx, src)
def _segment_sum(src, seg, n):
    out = torch.zeros((n,) + src.shape[1:], dtype=src.dtype)
    return out.index_add_(0, seg, src)
def _pad(x, pw):
    flat = tuple(v for pair in reversed(pw) for v in pair)
    return torch.nn.functional.pad(x, flat)

kernel_name = 'interleave_layers__gqa_block_cached__geglu_ffn__decode'

def setup_inputs():

    _w = (lambda a: (lambda t: t.to(torch.bfloat16) if t.is_floating_point() else t)(torch.as_tensor(a)))
    def _A_setup():
        B, Sq, D = 4, 1, 448
        Hq, Hkv, Sk, Dh = 16, 8, 4096, 256
        assert Hq % Hkv == 0, (Hq, Hkv)
        return {
            "x": _w((lambda *s: torch.randn(*s, dtype=torch.bfloat16) * 0.05)(B, Sq, D)),
            "k_cache": _w((lambda *s: torch.randn(*s, dtype=torch.bfloat16) * 1.6)(B, Hkv, Sk, Dh)),
            "v_cache": _w((lambda *s: torch.randn(*s, dtype=torch.bfloat16) * 1.6)(B, Hkv, Sk, Dh)),
            "wq": _w((lambda *s: torch.randn(*s, dtype=torch.bfloat16) * 1.6)(D, Hq * Dh) * (D ** -0.5)),
            "wo": _w((lambda *s: torch.randn(*s, dtype=torch.bfloat16) * 1.6)(Hq * Dh, D) * ((Hq * Dh) ** -0.5)),
        }
    _A_in = _A_setup()

    def _B_setup():
        B, S, D, F = 4, 1, 448, 1792
        return {
            "x": _w((lambda *s: torch.randn(*s, dtype=torch.bfloat16) * 0.05)(B, S, D)),
            "w_gate": _w((lambda *s: torch.randn(*s, dtype=torch.bfloat16) * 0.05)(D, F)),
            "w_up": _w((lambda *s: torch.randn(*s, dtype=torch.bfloat16) * 0.05)(D, F)),
            "w_down": _w((lambda *s: torch.randn(*s, dtype=torch.bfloat16) * 0.05)(F, D)),
        }
    _B_in = _B_setup()

    return {
        "A_x": _A_in["x"],
        "A_k_cache": _A_in["k_cache"],
        "A_v_cache": _A_in["v_cache"],
        "A_wq": _A_in["wq"],
        "A_wo": _A_in["wo"],
        "B_x": _B_in["x"],
        "B_w_gate": _B_in["w_gate"],
        "B_w_up": _B_in["w_up"],
        "B_w_down": _B_in["w_down"],
    }

def reference(A_x, A_k_cache, A_v_cache, A_wq, A_wo, B_x, B_w_gate, B_w_up, B_w_down):
    def _A_ref(x, k_cache, v_cache, wq, wo):
        B, Sq, D = x.shape
        Hq, Dh = 16, 256
        Hkv = k_cache.shape[1]
        rep = Hq // Hkv
        q = (lambda x, *d: x.permute(*d))((x @ wq).reshape(B, Sq, Hq, Dh), 0, 2, 1, 3)
        k = (lambda x, n, axis: torch.repeat_interleave(x, n, dim=axis))(k_cache, rep, axis=1)
        v = (lambda x, n, axis: torch.repeat_interleave(x, n, dim=axis))(v_cache, rep, axis=1)
        scores = torch.einsum("bhqd,bhkd->bhqk", q, k) * (Dh ** -0.5)
        p = _softmax(scores, axis=-1)
        o = torch.einsum("bhqk,bhkd->bhqd", p, v)
        return (lambda x, *d: x.permute(*d))(o, 0, 2, 1, 3).reshape(B, Sq, Hq * Dh) @ wo

    def _B_ref(x, w_gate, w_up, w_down):
        return (_gelu(x @ w_gate) * (x @ w_up)) @ w_down

    x = A_x
    x = x + _A_ref(_layernorm(x, axis=-1), A_k_cache, A_v_cache, A_wq, A_wo)
    x = x + _B_ref(_layernorm(x, axis=-1), B_w_gate, B_w_up, B_w_down)
    x = x + _A_ref(_layernorm(x, axis=-1), A_k_cache, A_v_cache, A_wq, A_wo)
    x = x + _B_ref(_layernorm(x, axis=-1), B_w_gate, B_w_up, B_w_down)
    return x
```

```cpp
#include <hip/hip_runtime.h>
#include <hip/hip_bf16.h>
#include <stdint.h>
typedef unsigned short u16;
typedef unsigned u32x4 __attribute__((ext_vector_type(4)));
__device__ __forceinline__ float bf2f(u16 v){ return __uint_as_float(((unsigned)v)<<16);}
__device__ __forceinline__ u16 f2bf(float f){ __hip_bfloat16 b = __float2bfloat16(f); return *reinterpret_cast<u16*>(&b);}
__device__ __forceinline__ float rbf(float f){ return bf2f(f2bf(f)); }
constexpr int B=4,D=448,HQ=16,HKV=8,SK=4096,DH=256,F=1792,NS=8;

__device__ void ln_rows(const float* x, float* xn  ) {
  int w = threadIdx.x>>6, l = threadIdx.x&63;
  float v[7]; float s=0;
  for(int i=0;i<7;i++){ v[i]=x[w*D+l+64*i]; s+=v[i]; }
  for(int o=32;o;o>>=1) s+=__shfl_xor(s,o);
  float m=s/D; float q=0;
  for(int i=0;i<7;i++){ float d=v[i]-m; q+=d*d; }
  for(int o=32;o;o>>=1) q+=__shfl_xor(q,o);
  float r=rsqrtf(q/D+1e-5f);
  for(int i=0;i<7;i++) xn[w*D+l+64*i]=rbf((v[i]-m)*r);
  __syncthreads();
}
__global__ void k_init(const u16* x, float* xf){ int i=blockIdx.x*256+threadIdx.x; if(i<B*D) xf[i]=bf2f(x[i]); }
__global__ void k_q(const float* x, const u16* wq, float* q){
  __shared__ float xn[B*D]; ln_rows(x,xn);
  int c=blockIdx.x*256+threadIdx.x; float a[4]={0,0,0,0};
  for(int k=0;k<D;k++){ float w=bf2f(wq[(size_t)k*4096+c]); for(int b=0;b<4;b++) a[b]+=xn[b*D+k]*w; }
  for(int b=0;b<4;b++) q[b*4096+c]=rbf(a[b]);
}
__global__ void k_attn(const float* q, const u16* K, const u16* V, float* po, float* pml){
  int s=blockIdx.x, kvh=blockIdx.y, b=blockIdx.z; int t=threadIdx.x, w=t>>6, l=t&63;
  __shared__ float sc[2][512]; __shared__ float red[2][4]; __shared__ float oacc[8][2][256];
  const u16* Kb=K+((size_t)(b*HKV+kvh)*SK+s*512)*DH; const u16* Vb=V+((size_t)(b*HKV+kvh)*SK+s*512)*DH;
  int c=l&31; float q0[8],q1[8];
  for(int e=0;e<8;e++){ q0[e]=q[b*4096+(kvh*2)*256+c*8+e]; q1[e]=q[b*4096+(kvh*2+1)*256+c*8+e]; }
  for(int i=0;i<64;i++){ int key=w*128+i*2+(l>>5);
    u32x4 kv=*(const u32x4*)(Kb+(size_t)key*DH+c*8); float d0=0,d1=0;
    for(int e=0;e<4;e++){ float lo=__uint_as_float(kv[e]<<16), hi=__uint_as_float(kv[e]&0xffff0000u); d0+=lo*q0[2*e]+hi*q0[2*e+1]; d1+=lo*q1[2*e]+hi*q1[2*e+1]; }
    for(int o=16;o;o>>=1){ d0+=__shfl_xor(d0,o); d1+=__shfl_xor(d1,o);}
    if(c==0){ sc[0][key]=d0*0.0625f; sc[1][key]=d1*0.0625f; } }
  __syncthreads();
  float m0=fmaxf(sc[0][t],sc[0][t+256]), m1=fmaxf(sc[1][t],sc[1][t+256]);
  for(int o=32;o;o>>=1){ m0=fmaxf(m0,__shfl_xor(m0,o)); m1=fmaxf(m1,__shfl_xor(m1,o)); }
  if(l==0){red[0][w]=m0;red[1][w]=m1;} __syncthreads();
  m0=fmaxf(fmaxf(red[0][0],red[0][1]),fmaxf(red[0][2],red[0][3])); m1=fmaxf(fmaxf(red[1][0],red[1][1]),fmaxf(red[1][2],red[1][3]));
  __syncthreads();
  float p00=__expf(sc[0][t]-m0),p01=__expf(sc[0][t+256]-m0),p10=__expf(sc[1][t]-m1),p11=__expf(sc[1][t+256]-m1);
  sc[0][t]=p00; sc[0][t+256]=p01; sc[1][t]=p10; sc[1][t+256]=p11;
  float l0=p00+p01,l1=p10+p11;
  for(int o=32;o;o>>=1){ l0+=__shfl_xor(l0,o); l1+=__shfl_xor(l1,o);}
  if(l==0){red[0][w]=l0;red[1][w]=l1;} __syncthreads();
  l0=red[0][0]+red[0][1]+red[0][2]+red[0][3]; l1=red[1][0]+red[1][1]+red[1][2]+red[1][3];
  float a0[8]={0},a1[8]={0}; int g=t>>5;
  for(int i=0;i<64;i++){ int key=g*64+i; u32x4 vv=*(const u32x4*)(Vb+(size_t)key*DH+c*8); float pa=sc[0][key],pb=sc[1][key];
    for(int e=0;e<4;e++){ float lo=__uint_as_float(vv[e]<<16), hi=__uint_as_float(vv[e]&0xffff0000u); a0[2*e]+=pa*lo; a0[2*e+1]+=pa*hi; a1[2*e]+=pb*lo; a1[2*e+1]+=pb*hi; } }
  for(int e=0;e<8;e++){ oacc[g][0][c*8+e]=a0[e]; oacc[g][1][c*8+e]=a1[e]; }
  __syncthreads();
  for(int h=0;h<2;h++){ float o=0; for(int gg=0;gg<8;gg++) o+=oacc[gg][h][t]; po[(((size_t)b*HQ+kvh*2+h)*NS+s)*DH+t]=o; }
  if(t==0){ size_t i0=((size_t)b*HQ+kvh*2)*NS+s, i1=i0+NS; pml[i0*2]=m0; pml[i0*2+1]=l0; pml[i1*2]=m1; pml[i1*2+1]=l1; }
}
__global__ void k_comb(const float* po,const float* pml,float* o){ int bh=blockIdx.x,t=threadIdx.x; float M=-1e30f; for(int s=0;s<NS;s++) M=fmaxf(M,pml[(bh*NS+s)*2]);
  float L=0,a=0; for(int s=0;s<NS;s++){ float w=__expf(pml[(bh*NS+s)*2]-M); L+=w*pml[(bh*NS+s)*2+1]; a+=w*po[((size_t)bh*NS+s)*DH+t]; } o[bh*DH+t]=rbf(a/L); }
template<int KK> __global__ void k_proj(const float* in,const u16* W,float* x, u16* out){
  __shared__ float red[32][4][8]; int c=threadIdx.x&7, ks=threadIdx.x>>3; int col=blockIdx.x*8+c; float a[4]={0,0,0,0};
  for(int k=ks;k<KK;k+=32){ float w=bf2f(W[(size_t)k*D+col]); for(int b=0;b<4;b++) a[b]+=in[b*KK+k]*w; }
  for(int b=0;b<4;b++) red[ks][b][c]=a[b]; __syncthreads();
  if(threadIdx.x<32){ int b=threadIdx.x>>3; float s=0; for(int i=0;i<32;i++) s+=red[i][b][c]; float r=rbf(x[b*D+col]+rbf(s)); x[b*D+col]=r; if(out) out[b*D+col]=f2bf(r);} }
__device__ __forceinline__ float gelu_t(float x){ float u=0.7978845608028654f*(x+0.044715f*x*x*x); return 0.5f*x*(1.f+tanhf(u)); }
__global__ void k_gu(const float* x,const u16* wg,const u16* wu,float* h){
  __shared__ float xn[B*D]; ln_rows(x,xn); int c=blockIdx.x*64+(threadIdx.x&63); int b=threadIdx.x>>6; float g=0,u=0;
  for(int k=0;k<D;k++){ float xv=xn[b*D+k]; g+=xv*bf2f(wg[(size_t)k*F+c]); u+=xv*bf2f(wu[(size_t)k*F+c]); }
  h[b*F+c]=rbf(rbf(gelu_t(rbf(g)))*rbf(u)); }

extern "C" void kernel_launch(void* const* d_in, const int* in_sizes, int n_in, void* d_out, int out_size, void* d_ws, size_t ws_size, hipStream_t stream){
  const u16* x=(const u16*)d_in[0]; const u16* K=(const u16*)d_in[1]; const u16* V=(const u16*)d_in[2]; const u16* wq=(const u16*)d_in[3]; const u16* wo=(const u16*)d_in[4];
  const u16* wg=(const u16*)d_in[6]; const u16* wu=(const u16*)d_in[7]; const u16* wd=(const u16*)d_in[8];
  float* ws=(float*)d_ws; float* xf=ws; float* q=ws+4096; float* o=q+16384; float* h=o+16384; float* pml=h+8192; float* po=pml+4096;
  k_init<<<7,256,0,stream>>>(x,xf);
  for(int L=0;L<2;L++){
    k_q<<<16,256,0,stream>>>(xf,wq,q);
    k_attn<<<dim3(NS,HKV,B),256,0,stream>>>(q,K,V,po,pml);
    k_comb<<<64,256,0,stream>>>(po,pml,o);
    k_proj<4096><<<56,256,0,stream>>>(o,wo,xf,nullptr);
    k_gu<<<28,256,0,stream>>>(xf,wg,wu,h);
    k_proj<1792><<<56,256,0,stream>>>(h,wd,xf,L==1?(u16*)d_out:nullptr);
  }
}
```

```cpp
#include <hip/hip_runtime.h>
#include <hip/hip_bf16.h>
#include <stdint.h>
typedef unsigned short u16;
typedef unsigned u32x4 __attribute__((ext_vector_type(4)));
__device__ __forceinline__ float bf2f(u16 v){ return __uint_as_float(((unsigned)v)<<16);}
__device__ __forceinline__ u16 f2bf(float f){ __hip_bfloat16 b = __float2bfloat16(f); return *reinterpret_cast<u16*>(&b);}
__device__ __forceinline__ float rbf(float f){ return bf2f(f2bf(f)); }
constexpr int B=4,D=448,HQ=16,HKV=8,SK=4096,DH=256,F=1792,NS=8;

__device__ void ln_rows(const float* x, float* xn  ) {
  int w = threadIdx.x>>6, l = threadIdx.x&63;
  float v[7]; float s=0;
  for(int i=0;i<7;i++){ v[i]=x[w*D+l+64*i]; s+=v[i]; }
  for(int o=32;o;o>>=1) s+=__shfl_xor(s,o);
  float m=s/D; float q=0;
  for(int i=0;i<7;i++){ float d=v[i]-m; q+=d*d; }
  for(int o=32;o;o>>=1) q+=__shfl_xor(q,o);
  float r=rsqrtf(q/D+1e-5f);
  for(int i=0;i<7;i++) xn[w*D+l+64*i]=rbf((v[i]-m)*r);
  __syncthreads();
}
__global__ void k_init(const u16* x, float* xf){ int i=blockIdx.x*256+threadIdx.x; if(i<B*D) xf[i]=bf2f(x[i]); }
__global__ void k_q(const float* x, const u16* wq, float* q){
  __shared__ float xn[B*D]; ln_rows(x,xn);
  int c=blockIdx.x*256+threadIdx.x; float a[4]={0,0,0,0};
  for(int k=0;k<D;k++){ float w=bf2f(wq[(size_t)k*4096+c]); for(int b=0;b<4;b++) a[b]+=xn[b*D+k]*w; }
  for(int b=0;b<4;b++) q[b*4096+c]=rbf(a[b]);
}
__global__ void k_attn(const float* q, const u16* K, const u16* V, float* po, float* pml){
  int s=blockIdx.x, kvh=blockIdx.y, b=blockIdx.z; int t=threadIdx.x, w=t>>6, l=t&63;
  __shared__ float sc[2][512]; __shared__ float red[2][4]; __shared__ float oacc[8][2][256];
  const u16* Kb=K+((size_t)(b*HKV+kvh)*SK+s*512)*DH; const u16* Vb=V+((size_t)(b*HKV+kvh)*SK+s*512)*DH;
  int c=l&31; float q0[8],q1[8];
  for(int e=0;e<8;e++){ q0[e]=q[b*4096+(kvh*2)*256+c*8+e]; q1[e]=q[b*4096+(kvh*2+1)*256+c*8+e]; }
  for(int i=0;i<64;i++){ int key=w*128+i*2+(l>>5);
    u32x4 kv=*(const u32x4*)(Kb+(size_t)key*DH+c*8); float d0=0,d1=0;
    for(int e=0;e<4;e++){ float lo=__uint_as_float(kv[e]<<16), hi=__uint_as_float(kv[e]&0xffff0000u); d0+=lo*q0[2*e]+hi*q0[2*e+1]; d1+=lo*q1[2*e]+hi*q1[2*e+1]; }
    for(int o=16;o;o>>=1){ d0+=__shfl_xor(d0,o); d1+=__shfl_xor(d1,o);}
    if(c==0){ sc[0][key]=d0*0.0625f; sc[1][key]=d1*0.0625f; } }
  __syncthreads();
  float m0=fmaxf(sc[0][t],sc[0][t+256]), m1=fmaxf(sc[1][t],sc[1][t+256]);
  for(int o=32;o;o>>=1){ m0=fmaxf(m0,__shfl_xor(m0,o)); m1=fmaxf(m1,__shfl_xor(m1,o)); }
  if(l==0){red[0][w]=m0;red[1][w]=m1;} __syncthreads();
  m0=fmaxf(fmaxf(red[0][0],red[0][1]),fmaxf(red[0][2],red[0][3])); m1=fmaxf(fmaxf(red[1][0],red[1][1]),fmaxf(red[1][2],red[1][3]));
  __syncthreads();
  float p00=__expf(sc[0][t]-m0),p01=__expf(sc[0][t+256]-m0),p10=__expf(sc[1][t]-m1),p11=__expf(sc[1][t+256]-m1);
  sc[0][t]=p00; sc[0][t+256]=p01; sc[1][t]=p10; sc[1][t+256]=p11;
  float l0=p00+p01,l1=p10+p11;
  for(int o=32;o;o>>=1){ l0+=__shfl_xor(l0,o); l1+=__shfl_xor(l1,o);}
  if(l==0){red[0][w]=l0;red[1][w]=l1;} __syncthreads();
  l0=red[0][0]+red[0][1]+red[0][2]+red[0][3]; l1=red[1][0]+red[1][1]+red[1][2]+red[1][3];
  float a0[8]={0},a1[8]={0}; int g=t>>5;
  for(int i=0;i<64;i++){ int key=g*64+i; u32x4 vv=*(const u32x4*)(Vb+(size_t)key*DH+c*8); float pa=sc[0][key],pb=sc[1][key];
    for(int e=0;e<4;e++){ float lo=__uint_as_float(vv[e]<<16), hi=__uint_as_float(vv[e]&0xffff0000u); a0[2*e]+=pa*lo; a0[2*e+1]+=pa*hi; a1[2*e]+=pb*lo; a1[2*e+1]+=pb*hi; } }
  for(int e=0;e<8;e++){ oacc[g][0][c*8+e]=a0[e]; oacc[g][1][c*8+e]=a1[e]; }
  __syncthreads();
  for(int h=0;h<2;h++){ float o=0; for(int gg=0;gg<8;gg++) o+=oacc[gg][h][t]; po[(((size_t)b*HQ+kvh*2+h)*NS+s)*DH+t]=o; }
  if(t==0){ size_t i0=((size_t)b*HQ+kvh*2)*NS+s, i1=i0+NS; pml[i0*2]=m0; pml[i0*2+1]=l0; pml[i1*2]=m1; pml[i1*2+1]=l1; }
}
__global__ void k_comb(const float* po,const float* pml,float* o){ int bh=blockIdx.x,t=threadIdx.x; float M=-1e30f; for(int s=0;s<NS;s++) M=fmaxf(M,pml[(bh*NS+s)*2]);
  float L=0,a=0; for(int s=0;s<NS;s++){ float w=__expf(pml[(bh*NS+s)*2]-M); L+=w*pml[(bh*NS+s)*2+1]; a+=w*po[((size_t)bh*NS+s)*DH+t]; } o[bh*DH+t]=rbf(a/L); }
template<int KK> __global__ void k_proj(const float* in,const u16* W,float* x, u16* out){
  __shared__ float red[32][4][8]; int c=threadIdx.x&7, ks=threadIdx.x>>3; int col=blockIdx.x*8+c; float a[4]={0,0,0,0};
  for(int k=ks;k<KK;k+=32){ float w=bf2f(W[(size_t)k*D+col]); for(int b=0;b<4;b++) a[b]+=in[b*KK+k]*w; }
  for(int b=0;b<4;b++) red[ks][b][c]=a[b]; __syncthreads();
  if(threadIdx.x<32){ int b=threadIdx.x>>3; float s=0; for(int i=0;i<32;i++) s+=red[i][b][c]; float r=rbf(x[b*D+col]+rbf(s)); x[b*D+col]=r; if(out) out[b*D+col]=f2bf(r);} }
__device__ __forceinline__ float gelu_t(float x){ float u=0.7978845608028654f*(x+0.044715f*x*x*x); return 0.5f*x*(1.f+tanhf(u)); }
__global__ void k_gu(const float* x,const u16* wg,const u16* wu,float* h){
  __shared__ float xn[B*D]; ln_rows(x,xn); int c=blockIdx.x*64+(threadIdx.x&63); int b=threadIdx.x>>6; float g=0,u=0;
  for(int k=0;k<D;k++){ float xv=xn[b*D+k]; g+=xv*bf2f(wg[(size_t)k*F+c]); u+=xv*bf2f(wu[(size_t)k*F+c]); }
  h[b*F+c]=rbf(rbf(gelu_t(rbf(g)))*rbf(u)); }

#define POL ""

#include <utility>
#define DI __device__ __forceinline__
template <int... Is, class F> DI void static_for_impl(std::integer_sequence<int, Is...>, F&& f) { (f(std::integral_constant<int, Is>{}), ...); }
template <int N, class F> DI void static_for(F&& f) { static_for_impl(std::make_integer_sequence<int, N>{}, f); }
template <int R> DI void lda(unsigned voff, __amdgpu_buffer_rsrc_t rs, unsigned soff) { asm volatile("buffer_load_dwordx4 a[%c3:%c4], %0, %1, %2 offen " POL :: "v"(voff), "s"(rs), "s"(soff), "i"(R), "i"(R + 3) : "memory"); }
template <int R> DI void ldv_(unsigned voff, __amdgpu_buffer_rsrc_t rs, unsigned soff) { asm volatile("buffer_load_dwordx4 v[%c3:%c4], %0, %1, %2 offen " POL :: "v"(voff), "s"(rs), "s"(soff), "i"(R), "i"(R + 3) : "memory"); }
DI void ldl(unsigned voff, __amdgpu_buffer_rsrc_t rs, unsigned soff, unsigned m0v) { asm volatile("s_mov_b32 m0, %3\n\ts_nop 0\n\tbuffer_load_dwordx4 %0, %1, %2 offen " POL " lds" :: "v"(voff), "s"(rs), "s"(soff), "s"(m0v) : "memory"); }
__global__ void __launch_bounds__(256, 1) __attribute__((amdgpu_num_vgpr(128))) k_stream(const unsigned char* K, const unsigned char* V, unsigned* sink) {
  __shared__ __attribute__((aligned(16))) unsigned char lds[4*37*1024];
  const int tid = threadIdx.x, lane = tid & 63, w = __builtin_amdgcn_readfirstlane(tid >> 6);
  int blk = blockIdx.x;
  { int x = blk & 7, r = blk >> 3; blk = ((r >> 3) * 8 + x) * 8 + (r & 7); }
  const size_t base = (size_t)blk * 262144;
  __amdgpu_buffer_rsrc_t rk = __builtin_amdgcn_make_buffer_rsrc((void*)(K + base), 0, 262144, 0x00020000);
  __amdgpu_buffer_rsrc_t rv = __builtin_amdgcn_make_buffer_rsrc((void*)(V + base), 0, 262144, 0x00020000);
  const unsigned kfr = (lane & 15) * 512 + (lane >> 4) * 16;
  const unsigned vfr = (lane >> 4) * 2048 + (lane & 15) * 16;
  asm volatile("" ::: "a255", "v255");
  constexpr int NLDS = 37;
  const unsigned ldsb = (unsigned)(size_t)lds + w * (NLDS * 1024);
  unsigned toff[8];
#pragma unroll
  for (int t = 0; t < 8; ++t) { int tt = ((t + (blockIdx.x >> 3)) & 7); toff[t] = (tt * 4 + w) * 8192; toff[t] = __builtin_amdgcn_readfirstlane(toff[t]); }
  auto ldk = [&](auto I) { constexpr int i = I.value;
    if constexpr (i < NLDS) ldl(kfr, rk, toff[i >> 3] + (i & 7) * 64, ldsb + i * 1024);
    else ldv_<148 + 4 * (i - NLDS)>(kfr, rk, toff[i >> 3] + (i & 7) * 64); };
  auto ldv = [&](auto I) { constexpr int i = I.value;
    lda<4 * i>(vfr, rv, toff[i >> 3] + ((i >> 1) & 3) * 512 + (i & 1) * 256); };
  asm volatile("s_nop 4");
  static_for<64>(ldk); static_for<64>(ldv);
  asm volatile("s_waitcnt vmcnt(0)" ::: "memory");
  unsigned x = 0;
  unsigned a; asm volatile("v_accvgpr_read_b32 %0, a255\n\tv_xor_b32 %0, %0, v255" : "=v"(a)); x ^= a;
  __syncthreads();
  x ^= ((unsigned*)lds)[tid];
  if (x == 0x12345678) sink[0] = x;
}

__global__ void k_stamp(unsigned long long* p){ p[0] = __builtin_amdgcn_s_memrealtime(); }
__global__ void k_wait(const unsigned long long* p, unsigned* sink){ unsigned long long t0 = p[0]; unsigned n = 0; while (__builtin_amdgcn_s_memrealtime() - t0 < 50000ull && n < 100000000u) { ++n; __builtin_amdgcn_s_sleep(1); } if (n == 0xffffffffu) sink[0] = n; }

extern "C" void kernel_launch(void* const* d_in, const int* in_sizes, int n_in, void* d_out, int out_size, void* d_ws, size_t ws_size, hipStream_t stream){
  const u16* x=(const u16*)d_in[0]; const u16* K=(const u16*)d_in[1]; const u16* V=(const u16*)d_in[2]; const u16* wq=(const u16*)d_in[3]; const u16* wo=(const u16*)d_in[4];
  const u16* wg=(const u16*)d_in[6]; const u16* wu=(const u16*)d_in[7]; const u16* wd=(const u16*)d_in[8];
  float* ws=(float*)d_ws; float* xf=ws; float* q=ws+4096; float* o=q+16384; float* h=o+16384; float* pml=h+8192; float* po=pml+4096;
  k_stamp<<<1,64,0,stream>>>((unsigned long long*)d_ws+(1<<20));
  k_init<<<7,256,0,stream>>>(x,xf);
  for(int L=0;L<2;L++){
    k_q<<<16,256,0,stream>>>(xf,wq,q);
    k_attn<<<dim3(NS,HKV,B),256,0,stream>>>(q,K,V,po,pml);
    k_comb<<<64,256,0,stream>>>(po,pml,o);
    k_proj<4096><<<56,256,0,stream>>>(o,wo,xf,nullptr);
    k_gu<<<28,256,0,stream>>>(xf,wg,wu,h);
    k_proj<1792><<<56,256,0,stream>>>(h,wd,xf,L==1?(u16*)d_out:nullptr);
  }
  k_wait<<<1,64,0,stream>>>((unsigned long long*)d_ws+(1<<20),(unsigned*)d_ws+(1<<20));
}
```

```cpp
#include <hip/hip_runtime.h>

__global__ void __launch_bounds__(256, 1)
fused_decode_kernel(const void* x, const void* kc, const void* vc, const void* wq, const void* wo,
                    const void* wg, const void* wu, const void* wd, void* out, void* ws) {
  __shared__ __attribute__((aligned(16))) unsigned char lds[163840];
  unsigned tid = threadIdx.x;
  unsigned blk = blockIdx.x;
  asm volatile(
    ".Lk_start:\n"
    "s_load_dwordx16 s[4:19], %1, 0x0\n"
    "s_load_dwordx4 s[20:23], %1, 0x40\n"
    "s_getpc_b64 s[30:31]\n"
    "v_and_b32 v29, 15, %0\n"
    "v_lshlrev_b32 v29, 7, v29\n"
    "global_load_dword v138, v29, s[30:31] offset:128\n"
    "s_and_b32 s65, %2, 7\n"
    "s_bfe_u32 s66, %2, 0x30003\n"
    "s_lshr_b32 s67, %2, 6\n"
    "v_lshrrev_b32 v20, 6, %0\n"
    "v_and_b32 v21, 63, %0\n"
    "v_readfirstlane_b32 s64, v20\n"
    "v_lshrrev_b32 v22, 4, v21\n"
    "v_and_b32 v23, 15, v21\n"
    "s_mov_b32 s69, 0\n"
    "s_lshl_b32 s71, s67, 6\n"
    "s_lshl_b32 s72, s65, 3\n"
    "s_add_u32 s71, s71, s72\n"
    "s_add_u32 s71, s71, s66\n"
    "s_lshl_b32 s71, s71, 18\n"
    "s_mov_b32 s74, s71\n"
    "s_mov_b32 s50, 0x40000\n"
    "s_mov_b32 s51, 0x20000\n"
    "s_mov_b32 s54, 0x40000\n"
    "s_mov_b32 s55, 0x20000\n"
    "s_lshl_b32 s72, s64, 14\n"
    "s_add_u32 s71, s66, 0\n"
    "s_and_b32 s71, s71, 3\n"
    "s_lshl_b32 s71, s71, 16\n"
    "s_add_u32 s76, s71, s72\n"
    "s_add_u32 s71, s66, 1\n"
    "s_and_b32 s71, s71, 3\n"
    "s_lshl_b32 s71, s71, 16\n"
    "s_add_u32 s77, s71, s72\n"
    "s_add_u32 s71, s66, 2\n"
    "s_and_b32 s71, s71, 3\n"
    "s_lshl_b32 s71, s71, 16\n"
    "s_add_u32 s78, s71, s72\n"
    "s_add_u32 s71, s66, 3\n"
    "s_and_b32 s71, s71, 3\n"
    "s_lshl_b32 s71, s71, 16\n"
    "s_add_u32 s79, s71, s72\n"
    "s_mul_i32 s75, s64, 36864\n"
    "s_add_u32 s75, s75, 0x4000\n"
    "v_lshrrev_b32 v36, 1, v23\n"
    "v_lshlrev_b32 v36, 10, v36\n"
    "v_lshl_add_u32 v36, v22, 5, v36\n"
    "v_and_b32 v39, 1, v23\n"
    "v_lshl_add_u32 v36, v39, 4, v36\n"
    "v_add_u32 v39, 0x2000, v36\n"
    "s_waitcnt lgkmcnt(0)\n"
    "s_add_u32 s48, s6, s74\n"
    "s_addc_u32 s49, s7, 0\n"
    "s_and_b32 s49, s49, 0xffff\n"
    "s_memrealtime s[92:93]\n"
    "s_add_u32 m0, s75, 0x0\n"
    "s_nop 0\n"
    "buffer_load_dwordx4 v36, s[48:51], s76 offen offset:0 nt lds\n"
    "s_add_u32 m0, s75, 0x380\n"
    "s_nop 0\n"
    "buffer_load_dwordx4 v36, s[48:51], s76 offen offset:128 nt lds\n"
    "buffer_load_dwordx4 a[0:3], v36, s[48:51], s76 offen offset:256 nt\n"
    "buffer_load_dwordx4 a[4:7], v36, s[48:51], s76 offen offset:384 nt\n"
    "buffer_load_dwordx4 a[16:19], v36, s[48:51], s76 offen offset:512 nt\n"
    "buffer_load_dwordx4 a[20:23], v36, s[48:51], s76 offen offset:640 nt\n"
    "buffer_load_dwordx4 a[24:27], v36, s[48:51], s76 offen offset:768 nt\n"
    "buffer_load_dwordx4 a[28:31], v36, s[48:51], s76 offen offset:896 nt\n"
    "buffer_load_dwordx4 a[40:43], v39, s[48:51], s76 offen offset:0 nt\n"
    "buffer_load_dwordx4 a[44:47], v39, s[48:51], s76 offen offset:128 nt\n"
    "s_add_u32 m0, s75, 0x1700\n"
    "s_nop 0\n"
    "buffer_load_dwordx4 v39, s[48:51], s76 offen offset:256 nt lds\n"
    "s_add_u32 m0, s75, 0x1a80\n"
    "s_nop 0\n"
    "buffer_load_dwordx4 v39, s[48:51], s76 offen offset:384 nt lds\n"
    "s_add_u32 s52, s8, s74\n"
    "s_addc_u32 s53, s9, 0\n"
    "s_and_b32 s53, s53, 0xffff\n"
    "s_lshl_b32 s71, s67, 21\n"
    "s_mov_b64 s[32:33], s[22:23]\n"
    "s_add_u32 s32, s32, s71\n"
    "s_addc_u32 s33, s33, 0\n"
    "s_add_u32 s94, s32, 0x121000\n"
    "s_addc_u32 s95, s33, 0\n"
    "s_load_dword s68, s[94:95], 0x0\n"
    "s_and_b32 s33, s33, 0xffff\n"
    "s_mov_b32 s34, 0x200000\n"
    "s_mov_b32 s35, 0x20000\n"
    "v_lshlrev_b32 v1, 4, v21\n"
    "v_lshlrev_b32 v19, 3, v21\n"
    "v_lshrrev_b32 v24, 1, v22\n"
    "v_and_b32 v25, 1, v22\n"
    "v_lshlrev_b32 v26, 4, v23\n"
    "v_and_b32 v27, 7, v23\n"
    "v_lshrrev_b32 v28, 3, v23\n"
    "s_getpc_b64 s[94:95]\n"
    ".Lk_pc:\n"
    "s_lshr_b32 s71, %2, 3\n"
    "s_lshl_b32 s71, s71, 2\n"
    "s_add_u32 s71, s71, s64\n"
    "s_lshl_b32 s71, s71, 8\n"
    "v_and_b32 v29, 1, v21\n"
    "v_lshl_add_u32 v29, v29, 7, s71\n"
    "v_min_u32 v29, .Lk_end-.Lk_pc, v29\n"
    "global_load_dword v143, v29, s[94:95]\n"
    "v_lshl_add_u32 v37, v24, 13, v26\n"
    "v_lshl_add_u32 v37, v25, 9, v37\n"
    "v_add_u32 v38, 0x1000, v37\n"
    "v_cmp_eq_u32 vcc, 0, v28\n"
    "v_mov_b32 v29, 0xffff0000\n"
    "v_mov_b32 v31, 0xffff\n"
    "v_cndmask_b32 v29, v29, v31, vcc\n"
    "v_cmp_gt_u32 vcc, 4, v27\n"
    "s_nop 1\n"
    "v_cndmask_b32 v2, 0, v29, vcc\n"
    "v_cndmask_b32 v3, v29, 0, vcc\n"
    "v_and_b32 v31, 3, v23\n"
    "v_mul_u32_u24 v4, 0x5555, v31\n"
    "v_bfe_u32 v32, v23, 1, 1\n"
    "v_mul_u32_u24 v29, 0xaaaa, v32\n"
    "v_add_u32 v29, 0x4444, v29\n"
    "v_lshl_or_b32 v4, v29, 16, v4\n"
    "v_lshl_add_u32 v8, v24, 17, v26\n"
    "v_lshl_add_u32 v8, v25, 13, v8\n"
    "v_lshl_add_u32 v29, v24, 5, v28\n"
    "v_lshl_add_u32 v29, v25, 1, v29\n"
    "v_mul_u32_u24 v29, 0x380, v29\n"
    "v_lshl_add_u32 v9, v27, 4, v29\n"
    "v_lshl_add_u32 v29, v24, 3, v25\n"
    "v_mul_u32_u24 v29, 0x380, v29\n"
    "v_add_u32 v12, v29, v26\n"
    "v_lshlrev_b32 v13, 5, v23\n"
    "v_lshl_add_u32 v13, v25, 4, v13\n"
    "s_lshl_b32 s71, s64, 9\n"
    "v_xor_b32 v29, v24, v28\n"
    "v_lshl_add_u32 v29, v29, 11, s71\n"
    "v_lshl_add_u32 v29, v24, 8, v29\n"
    "v_lshl_add_u32 v29, v27, 5, v29\n"
    "v_lshl_add_u32 v15, v25, 4, v29\n"
    "s_mul_i32 s74, s65, 7\n"
    "s_add_u32 s74, s74, s66\n"
    "s_min_u32 s74, s74, 55\n"
    "v_lshl_add_u32 v33, v22, 2, v28\n"
    "v_min_u32 v29, 13, v23\n"
    "v_and_b32 v30, 1, v29\n"
    "v_lshrrev_b32 v29, 1, v29\n"
    "v_lshl_add_u32 v30, v30, 1, v24\n"
    "v_lshl_add_u32 v30, v25, 2, v30\n"
    "v_lshl_add_u32 v34, v29, 3, v30\n"
    "v_sub_u32 v31, 6, v29\n"
    "v_lshl_add_u32 v31, v31, 3, v30\n"
    "s_and_b32 s71, s74, 1\n"
    "s_cmp_eq_u32 s71, 1\n"
    "s_cselect_b64 vcc, -1, 0\n"
    "v_cndmask_b32 v31, v34, v31, vcc\n"
    "v_lshlrev_b32 v14, 3, v31\n"
    "s_mov_b32 s60, 0xe000\n"
    "s_cselect_b32 s60, 0xffff2000, s60\n"
    "s_cselect_b32 s61, -1, 0\n"
    "s_cselect_b32 s72, 97, 1\n"
    "s_cmp_eq_u32 s66, 7\n"
    "s_cselect_b32 s60, 0, s60\n"
    "s_cselect_b32 s61, 0, s61\n"
    "s_mul_i32 s71, s64, 112\n"
    "s_add_u32 s71, s71, s72\n"
    "v_add_u32 v29, s71, v33\n"
    "v_mul_u32_u24 v29, 0xe00, v29\n"
    "s_lshl_b32 s71, s74, 6\n"
    "v_and_b32 v30, 3, v23\n"
    "v_lshl_add_u32 v30, v30, 4, s71\n"
    "v_add_u32 v40, v29, v30\n"
    "v_mov_b32 v41, 0\n"
    "v_mov_b64 v[10:11], s[14:15]\n"
    "s_mov_b32 exec_lo, 0xf0f0f0f0\n"
    "s_mov_b32 exec_hi, 0xf0f0f0f0\n"
    "v_mov_b64 v[10:11], s[16:17]\n"
    "s_mov_b64 exec, -1\n"
    "v_lshl_add_u64 v[10:11], v[10:11], 0, v[40:41]\n"
    "s_lshr_b32 s73, s64, 1\n"
    "s_lshl_b32 s71, s73, 9\n"
    "s_and_b32 s72, s64, 1\n"
    "s_lshl_b32 s72, s72, 6\n"
    "s_add_u32 s71, s71, s72\n"
    "s_add_u32 s71, s71, 0x3800\n"
    "s_lshl_b32 s73, s73, 1\n"
    "v_and_b32 v30, 1, v21\n"
    "v_lshl_add_u32 v29, v30, 8, s71\n"
    "v_or_b32 v30, s73, v30\n"
    "v_xor_b32 v30, v30, v22\n"
    "v_lshl_add_u32 v29, v30, 4, v29\n"
    "v_bfe_u32 v30, v21, 2, 1\n"
    "v_lshl_add_u32 v29, v30, 7, v29\n"
    "v_lshl_add_u32 v29, v28, 3, v29\n"
    "v_lshl_add_u32 v16, v32, 2, v29\n"
    "v_lshrrev_b32 v29, 1, v27\n"
    "v_xor_b32 v29, v29, v22\n"
    "v_lshlrev_b32 v29, 4, v29\n"
    "v_lshl_add_u32 v29, v27, 7, v29\n"
    "v_add_u32 v17, 0x3800, v29\n"
    "s_lshl_b32 s71, s64, 11\n"
    "s_add_u32 s71, s71, 0x1000\n"
    "v_lshl_add_u32 v29, v24, 10, v13\n"
    "v_add_u32 v18, s71, v29\n"
    "s_lshl_b32 s71, s65, 2\n"
    "s_add_u32 s71, s71, s64\n"
    "s_mul_i32 s71, s71, 0x1c000\n"
    "s_lshl_b32 s72, s66, 7\n"
    "s_add_u32 s71, s71, s72\n"
    "s_mov_b64 s[40:41], s[12:13]\n"
    "s_add_u32 s40, s40, s71\n"
    "s_addc_u32 s41, s41, 0\n"
    "s_sub_u32 s42, 0x380000, s71\n"
    "s_and_b32 s41, s41, 0xffff\n"
    "s_mov_b32 s43, 0x20000\n"
    "s_cmp_eq_u32 s66, 7\n"
    "s_cselect_b32 s42, 0, s42\n"
    "s_mul_i32 s71, s74, 0x7000\n"
    "s_lshl_b32 s72, s64, 8\n"
    "s_add_u32 s71, s71, s72\n"
    "s_mul_i32 s72, s65, 0x31000\n"
    "s_lshl_b32 s73, s67, 2\n"
    "s_add_u32 s73, s73, s64\n"
    "s_mul_i32 s73, s73, 0x3100\n"
    "s_add_u32 s72, s72, s73\n"
    "s_cmp_eq_u32 s66, 7\n"
    "s_cselect_b32 s71, s72, s71\n"
    "s_mov_b64 s[44:45], s[18:19]\n"
    "s_add_u32 s44, s44, s71\n"
    "s_addc_u32 s45, s45, 0\n"
    "s_sub_u32 s46, 0x188000, s71\n"
    "s_and_b32 s45, s45, 0xffff\n"
    "s_mov_b32 s47, 0x20000\n"
    "s_lshl_b32 s71, s66, 19\n"
    "s_lshl_b32 s72, s65, 10\n"
    "s_add_u32 s71, s71, s72\n"
    "s_lshl_b32 s72, s64, 8\n"
    "s_add_u32 s71, s71, s72\n"
    "s_min_u32 s71, s71, 0x380000\n"
    "s_mov_b64 s[36:37], s[10:11]\n"
    "s_add_u32 s36, s36, s71\n"
    "s_addc_u32 s37, s37, 0\n"
    "s_sub_u32 s38, 0x380000, s71\n"
    "s_and_b32 s37, s37, 0xffff\n"
    "s_mov_b32 s39, 0x20000\n"
    "s_mul_i32 s71, s64, 0x7000\n"
    "s_lshl_b32 s72, s74, 8\n"
    "s_add_u32 s71, s71, s72\n"
    "s_add_u32 s90, s71, 0x50000\n"
    "s_mul_i32 s71, s66, 0x3800\n"
    "s_mul_i32 s72, s64, 0xe00\n"
    "s_add_u32 s71, s71, s72\n"
    "s_add_u32 s91, s71, 0x50000\n"
    "s_lshl_b32 s71, s65, 2\n"
    "s_add_u32 s71, s71, s64\n"
    "s_lshl_b32 s82, s71, 12\n"
    "s_lshl_b32 s72, s71, 10\n"
    "s_add_u32 s83, s72, 0x20000\n"
    "s_add_u32 s86, s82, 0x28000\n"
    "s_add_u32 s87, s72, 0x48000\n"
    "s_lshl_b32 s72, s66, 9\n"
    "s_add_u32 s84, s86, s72\n"
    "s_add_u32 s80, s82, s72\n"
    "s_lshl_b32 s72, s66, 7\n"
    "s_add_u32 s85, s87, s72\n"
    "s_add_u32 s81, s83, s72\n"
    "s_lshl_b32 s71, s65, 11\n"
    "s_lshl_b32 s72, s66, 8\n"
    "s_add_u32 s71, s71, s72\n"
    "s_lshl_b32 s72, s64, 15\n"
    "s_add_u32 s71, s71, s72\n"
    "s_add_u32 s88, s71, 0x70000\n"
    "s_mul_i32 s71, s64, 0x1c0\n"
    "s_and_b32 s72, s65, 7\n"
    "s_lshl_b32 s72, s72, 14\n"
    "s_add_u32 s71, s71, s72\n"
    "s_add_u32 s89, s71, 0x70000\n"
    "s_mov_b32 s70, 0\n"
    "s_mov_b32 s62, 0x3f80\n"
    "s_mov_b32 s63, 0x3f800000\n"
    "s_mov_b32 s96, 1\n"
    "s_mov_b32 s97, 1\n"
    "s_mov_b32 s98, 0x3fff3fff\n"
    "s_mov_b32 s99, 0x3fff3fff\n"
    "s_mul_i32 s71, s67, 896\n"
    "s_mov_b64 s[56:57], s[4:5]\n"
    "s_add_u32 s56, s56, s71\n"
    "s_addc_u32 s57, s57, 0\n"
    "s_sub_u32 s58, 0xe00, s71\n"
    "s_and_b32 s57, s57, 0xffff\n"
    "s_mov_b32 s59, 0x20000\n"
    "s_mul_i32 s71, s64, 224\n"
    "v_lshlrev_b32 v34, 2, v34\n"
    "buffer_load_dword v35, v34, s[56:59], s71 offen\n"
    "s_lshl_b32 s71, s66, 7\n"
    "v_and_b32 v29, 31, v21\n"
    "v_lshlrev_b32 v29, 2, v29\n"
    "buffer_load_dword v7, v29, s[56:59], s71 offen\n"
    "s_mul_i32 s71, s64, 0xe0000\n"
    "s_lshl_b32 s72, s65, 10\n"
    "s_add_u32 s71, s71, s72\n"
    "s_xor_b32 s72, s66, 4\n"
    "s_lshl_b32 s72, s72, 7\n"
    "s_add_u32 s71, s71, s72\n"
    "s_mov_b64 s[56:57], s[10:11]\n"
    "s_add_u32 s56, s56, s71\n"
    "s_addc_u32 s57, s57, 0\n"
    "s_sub_u32 s58, 0x380000, s71\n"
    "s_and_b32 s57, s57, 0xffff\n"
    "s_mov_b32 s59, 0x20000\n"
    "v_lshlrev_b32 v34, 13, v33\n"
    "v_lshl_add_u32 v34, v27, 4, v34\n"
    "s_waitcnt lgkmcnt(0)\n"
    "s_add_u32 s68, s68, 1\n"
    "s_lshr_b32 s71, %2, 3\n"
    "s_mul_i32 s71, s71, 24\n"
    "s_lshr_b32 s71, s71, 4\n"
    "s_sub_u32 s92, s92, s71\n"
    "s_lshr_b32 s71, %2, 6\n"
    "s_mul_i32 s71, s71, 20\n"
    "s_sub_u32 s92, s92, s71\n"
    "s_cmp_lg_u32 s67, 0\n"
    "s_cbranch_scc1 .Lk_u1\n"
    "buffer_load_dwordx4 v[56:59], v34, s[56:59], 0 offen offset:0\n"
    "s_mov_b32 s71, 0x4000\n"
    "buffer_load_dwordx4 v[60:63], v34, s[56:59], s71 offen offset:0\n"
    "s_mov_b32 s72, 0x20000\n"
    "buffer_load_dwordx4 v[64:67], v34, s[56:59], s72 offen offset:0\n"
    "s_mov_b32 s71, 0x24000\n"
    "buffer_load_dwordx4 v[68:71], v34, s[56:59], s71 offen offset:0\n"
    "s_mov_b32 s72, 0x40000\n"
    "buffer_load_dwordx4 v[72:75], v34, s[56:59], s72 offen offset:0\n"
    "s_mov_b32 s71, 0x44000\n"
    "buffer_load_dwordx4 v[76:79], v34, s[56:59], s71 offen offset:0\n"
    "s_mov_b32 s72, 0x60000\n"
    "buffer_load_dwordx4 v[80:83], v34, s[56:59], s72 offen offset:0\n"
    "s_mov_b32 s71, 0x64000\n"
    "buffer_load_dwordx4 v[84:87], v34, s[56:59], s71 offen offset:0\n"
    "s_mov_b32 s72, 0x80000\n"
    "buffer_load_dwordx4 v[88:91], v34, s[56:59], s72 offen offset:0\n"
    "s_mov_b32 s71, 0x84000\n"
    "buffer_load_dwordx4 v[92:95], v34, s[56:59], s71 offen offset:0\n"
    "s_mov_b32 s72, 0xa0000\n"
    "buffer_load_dwordx4 v[96:99], v34, s[56:59], s72 offen offset:0\n"
    "s_mov_b32 s71, 0xa4000\n"
    "buffer_load_dwordx4 v[100:103], v34, s[56:59], s71 offen offset:0\n"
    "s_mov_b32 s72, 0xc0000\n"
    "buffer_load_dwordx4 v[104:107], v34, s[56:59], s72 offen offset:0\n"
    "s_mov_b32 s71, 0xc4000\n"
    "buffer_load_dwordx4 v[108:111], v34, s[56:59], s71 offen offset:0\n"
    ".Lk_u1:\n"
    ".Lk_u16:\n"
    "s_memrealtime s[100:101]\n"
    "s_waitcnt lgkmcnt(0)\n"
    "s_sub_u32 s100, s100, s92\n"
    "s_cmp_lt_u32 s100, 178\n"
    "s_cbranch_scc1 .Lk_u16\n"
    "s_add_u32 m0, s75, 0x1e00\n"
    "s_nop 0\n"
    "buffer_load_dwordx4 v39, s[48:51], s76 offen offset:512 nt lds\n"
    "s_add_u32 m0, s75, 0x2180\n"
    "s_nop 0\n"
    "buffer_load_dwordx4 v39, s[48:51], s76 offen offset:640 nt lds\n"
    "buffer_load_dwordx4 a[64:67], v39, s[48:51], s76 offen offset:768 nt\n"
    "buffer_load_dwordx4 a[68:71], v39, s[48:51], s76 offen offset:896 nt\n"
    "s_cmp_lg_u32 s67, 1\n"
    "s_cbranch_scc1 .Lk_u17\n"
    "buffer_load_dwordx4 v[56:59], v34, s[56:59], 0 offen offset:0\n"
    "s_mov_b32 s71, 0x4000\n"
    "buffer_load_dwordx4 v[60:63], v34, s[56:59], s71 offen offset:0\n"
    "s_mov_b32 s72, 0x20000\n"
    "buffer_load_dwordx4 v[64:67], v34, s[56:59], s72 offen offset:0\n"
    "s_mov_b32 s71, 0x24000\n"
    "buffer_load_dwordx4 v[68:71], v34, s[56:59], s71 offen offset:0\n"
    "s_mov_b32 s72, 0x40000\n"
    "buffer_load_dwordx4 v[72:75], v34, s[56:59], s72 offen offset:0\n"
    "s_mov_b32 s71, 0x44000\n"
    "buffer_load_dwordx4 v[76:79], v34, s[56:59], s71 offen offset:0\n"
    "s_mov_b32 s72, 0x60000\n"
    "buffer_load_dwordx4 v[80:83], v34, s[56:59], s72 offen offset:0\n"
    "s_mov_b32 s71, 0x64000\n"
    "buffer_load_dwordx4 v[84:87], v34, s[56:59], s71 offen offset:0\n"
    "s_mov_b32 s72, 0x80000\n"
    "buffer_load_dwordx4 v[88:91], v34, s[56:59], s72 offen offset:0\n"
    "s_mov_b32 s71, 0x84000\n"
    "buffer_load_dwordx4 v[92:95], v34, s[56:59], s71 offen offset:0\n"
    "s_mov_b32 s72, 0xa0000\n"
    "buffer_load_dwordx4 v[96:99], v34, s[56:59], s72 offen offset:0\n"
    "s_mov_b32 s71, 0xa4000\n"
    "buffer_load_dwordx4 v[100:103], v34, s[56:59], s71 offen offset:0\n"
    "s_mov_b32 s72, 0xc0000\n"
    "buffer_load_dwordx4 v[104:107], v34, s[56:59], s72 offen offset:0\n"
    "s_mov_b32 s71, 0xc4000\n"
    "buffer_load_dwordx4 v[108:111], v34, s[56:59], s71 offen offset:0\n"
    ".Lk_u17:\n"
    ".Lk_u32:\n"
    "s_memrealtime s[100:101]\n"
    "s_waitcnt lgkmcnt(0)\n"
    "s_sub_u32 s100, s100, s92\n"
    "s_cmp_lt_u32 s100, 253\n"
    "s_cbranch_scc1 .Lk_u32\n"
    "buffer_load_dwordx4 a[8:11], v36, s[48:51], s77 offen offset:0 nt\n"
    "buffer_load_dwordx4 a[12:15], v36, s[48:51], s77 offen offset:128 nt\n"
    "s_add_u32 m0, s75, 0x700\n"
    "s_nop 0\n"
    "buffer_load_dwordx4 v36, s[48:51], s77 offen offset:256 nt lds\n"
    "s_add_u32 m0, s75, 0xa80\n"
    "s_nop 0\n"
    "buffer_load_dwordx4 v36, s[48:51], s77 offen offset:384 nt lds\n"
    "s_cmp_lg_u32 s67, 2\n"
    "s_cbranch_scc1 .Lk_u33\n"
    "buffer_load_dwordx4 v[56:59], v34, s[56:59], 0 offen offset:0\n"
    "s_mov_b32 s71, 0x4000\n"
    "buffer_load_dwordx4 v[60:63], v34, s[56:59], s71 offen offset:0\n"
    "s_mov_b32 s72, 0x20000\n"
    "buffer_load_dwordx4 v[64:67], v34, s[56:59], s72 offen offset:0\n"
    "s_mov_b32 s71, 0x24000\n"
    "buffer_load_dwordx4 v[68:71], v34, s[56:59], s71 offen offset:0\n"
    "s_mov_b32 s72, 0x40000\n"
    "buffer_load_dwordx4 v[72:75], v34, s[56:59], s72 offen offset:0\n"
    "s_mov_b32 s71, 0x44000\n"
    "buffer_load_dwordx4 v[76:79], v34, s[56:59], s71 offen offset:0\n"
    "s_mov_b32 s72, 0x60000\n"
    "buffer_load_dwordx4 v[80:83], v34, s[56:59], s72 offen offset:0\n"
    "s_mov_b32 s71, 0x64000\n"
    "buffer_load_dwordx4 v[84:87], v34, s[56:59], s71 offen offset:0\n"
    "s_mov_b32 s72, 0x80000\n"
    "buffer_load_dwordx4 v[88:91], v34, s[56:59], s72 offen offset:0\n"
    "s_mov_b32 s71, 0x84000\n"
    "buffer_load_dwordx4 v[92:95], v34, s[56:59], s71 offen offset:0\n"
    "s_mov_b32 s72, 0xa0000\n"
    "buffer_load_dwordx4 v[96:99], v34, s[56:59], s72 offen offset:0\n"
    "s_mov_b32 s71, 0xa4000\n"
    "buffer_load_dwordx4 v[100:103], v34, s[56:59], s71 offen offset:0\n"
    "s_mov_b32 s72, 0xc0000\n"
    "buffer_load_dwordx4 v[104:107], v34, s[56:59], s72 offen offset:0\n"
    "s_mov_b32 s71, 0xc4000\n"
    "buffer_load_dwordx4 v[108:111], v34, s[56:59], s71 offen offset:0\n"
    ".Lk_u33:\n"
    ".Lk_u48:\n"
    "s_memrealtime s[100:101]\n"
    "s_waitcnt lgkmcnt(0)\n"
    "s_sub_u32 s100, s100, s92\n"
    "s_cmp_lt_u32 s100, 327\n"
    "s_cbranch_scc1 .Lk_u48\n"
    "s_add_u32 m0, s75, 0xe00\n"
    "s_nop 0\n"
    "buffer_load_dwordx4 v36, s[48:51], s77 offen offset:512 nt lds\n"
    "s_add_u32 m0, s75, 0x1180\n"
    "s_nop 0\n"
    "buffer_load_dwordx4 v36, s[48:51], s77 offen offset:640 nt lds\n"
    "buffer_load_dwordx4 a[32:35], v36, s[48:51], s77 offen offset:768 nt\n"
    "buffer_load_dwordx4 a[36:39], v36, s[48:51], s77 offen offset:896 nt\n"
    "s_cmp_lg_u32 s67, 3\n"
    "s_cbranch_scc1 .Lk_u49\n"
    "buffer_load_dwordx4 v[56:59], v34, s[56:59], 0 offen offset:0\n"
    "s_mov_b32 s71, 0x4000\n"
    "buffer_load_dwordx4 v[60:63], v34, s[56:59], s71 offen offset:0\n"
    "s_mov_b32 s72, 0x20000\n"
    "buffer_load_dwordx4 v[64:67], v34, s[56:59], s72 offen offset:0\n"
    "s_mov_b32 s71, 0x24000\n"
    "buffer_load_dwordx4 v[68:71], v34, s[56:59], s71 offen offset:0\n"
    "s_mov_b32 s72, 0x40000\n"
    "buffer_load_dwordx4 v[72:75], v34, s[56:59], s72 offen offset:0\n"
    "s_mov_b32 s71, 0x44000\n"
    "buffer_load_dwordx4 v[76:79], v34, s[56:59], s71 offen offset:0\n"
    "s_mov_b32 s72, 0x60000\n"
    "buffer_load_dwordx4 v[80:83], v34, s[56:59], s72 offen offset:0\n"
    "s_mov_b32 s71, 0x64000\n"
    "buffer_load_dwordx4 v[84:87], v34, s[56:59], s71 offen offset:0\n"
    "s_mov_b32 s72, 0x80000\n"
    "buffer_load_dwordx4 v[88:91], v34, s[56:59], s72 offen offset:0\n"
    "s_mov_b32 s71, 0x84000\n"
    "buffer_load_dwordx4 v[92:95], v34, s[56:59], s71 offen offset:0\n"
    "s_mov_b32 s72, 0xa0000\n"
    "buffer_load_dwordx4 v[96:99], v34, s[56:59], s72 offen offset:0\n"
    "s_mov_b32 s71, 0xa4000\n"
    "buffer_load_dwordx4 v[100:103], v34, s[56:59], s71 offen offset:0\n"
    "s_mov_b32 s72, 0xc0000\n"
    "buffer_load_dwordx4 v[104:107], v34, s[56:59], s72 offen offset:0\n"
    "s_mov_b32 s71, 0xc4000\n"
    "buffer_load_dwordx4 v[108:111], v34, s[56:59], s71 offen offset:0\n"
    ".Lk_u49:\n"
    ".Lk_u64:\n"
    "s_memrealtime s[100:101]\n"
    "s_waitcnt lgkmcnt(0)\n"
    "s_sub_u32 s100, s100, s92\n"
    "s_cmp_lt_u32 s100, 401\n"
    "s_cbranch_scc1 .Lk_u64\n"
    "buffer_load_dwordx4 a[48:51], v39, s[48:51], s77 offen offset:0 nt\n"
    "buffer_load_dwordx4 a[52:55], v39, s[48:51], s77 offen offset:128 nt\n"
    "buffer_load_dwordx4 a[56:59], v39, s[48:51], s77 offen offset:256 nt\n"
    "buffer_load_dwordx4 a[60:63], v39, s[48:51], s77 offen offset:384 nt\n"
    ".Lk_u65:\n"
    "s_memrealtime s[100:101]\n"
    "s_waitcnt lgkmcnt(0)\n"
    "s_sub_u32 s100, s100, s92\n"
    "s_cmp_lt_u32 s100, 475\n"
    "s_cbranch_scc1 .Lk_u65\n"
    "buffer_load_dwordx4 a[72:75], v39, s[48:51], s77 offen offset:512 nt\n"
    "buffer_load_dwordx4 a[76:79], v39, s[48:51], s77 offen offset:640 nt\n"
    "s_add_u32 m0, s75, 0x2500\n"
    "s_nop 0\n"
    "buffer_load_dwordx4 v39, s[48:51], s77 offen offset:768 nt lds\n"
    "s_add_u32 m0, s75, 0x2880\n"
    "s_nop 0\n"
    "buffer_load_dwordx4 v39, s[48:51], s77 offen offset:896 nt lds\n"
    ".Lk_u66:\n"
    "s_memrealtime s[100:101]\n"
    "s_waitcnt lgkmcnt(0)\n"
    "s_sub_u32 s100, s100, s92\n"
    "s_cmp_lt_u32 s100, 536\n"
    "s_cbranch_scc1 .Lk_u66\n"
    "buffer_load_dwordx4 a[80:83], v36, s[48:51], s78 offen offset:0 nt\n"
    "buffer_load_dwordx4 a[84:87], v36, s[48:51], s78 offen offset:128 nt\n"
    "buffer_load_dwordx4 a[88:91], v36, s[48:51], s78 offen offset:256 nt\n"
    "buffer_load_dwordx4 a[92:95], v36, s[48:51], s78 offen offset:384 nt\n"
    ".Lk_u67:\n"
    "s_memrealtime s[100:101]\n"
    "s_waitcnt lgkmcnt(0)\n"
    "s_sub_u32 s100, s100, s92\n"
    "s_cmp_lt_u32 s100, 597\n"
    "s_cbranch_scc1 .Lk_u67\n"
    "buffer_load_dwordx4 a[104:107], v36, s[48:51], s78 offen offset:512 nt\n"
    "buffer_load_dwordx4 a[108:111], v36, s[48:51], s78 offen offset:640 nt\n"
    "s_add_u32 m0, s75, 0x3500\n"
    "s_nop 0\n"
    "buffer_load_dwordx4 v36, s[48:51], s78 offen offset:768 nt lds\n"
    "s_add_u32 m0, s75, 0x3880\n"
    "s_nop 0\n"
    "buffer_load_dwordx4 v36, s[48:51], s78 offen offset:896 nt lds\n"
    ".Lk_u68:\n"
    "s_memrealtime s[100:101]\n"
    "s_waitcnt lgkmcnt(0)\n"
    "s_sub_u32 s100, s100, s92\n"
    "s_cmp_lt_u32 s100, 658\n"
    "s_cbranch_scc1 .Lk_u68\n"
    "s_add_u32 m0, s75, 0x4000\n"
    "s_nop 0\n"
    "buffer_load_dwordx4 v39, s[48:51], s78 offen offset:0 nt lds\n"
    "s_add_u32 m0, s75, 0x4380\n"
    "s_nop 0\n"
    "buffer_load_dwordx4 v39, s[48:51], s78 offen offset:128 nt lds\n"
    "buffer_load_dwordx4 a[128:131], v39, s[48:51], s78 offen offset:256 nt\n"
    "buffer_load_dwordx4 a[132:135], v39, s[48:51], s78 offen offset:384 nt\n"
    ".Lk_u69:\n"
    "s_memrealtime s[100:101]\n"
    "s_waitcnt lgkmcnt(0)\n"
    "s_sub_u32 s100, s100, s92\n"
    "s_cmp_lt_u32 s100, 719\n"
    "s_cbranch_scc1 .Lk_u69\n"
    "buffer_load_dwordx4 a[144:147], v39, s[48:51], s78 offen offset:512 nt\n"
    "buffer_load_dwordx4 a[148:151], v39, s[48:51], s78 offen offset:640 nt\n"
    "buffer_load_dwordx4 a[152:155], v39, s[48:51], s78 offen offset:768 nt\n"
    "buffer_load_dwordx4 a[156:159], v39, s[48:51], s78 offen offset:896 nt\n"
    ".Lk_u70:\n"
    "s_memrealtime s[100:101]\n"
    "s_waitcnt lgkmcnt(0)\n"
    "s_sub_u32 s100, s100, s92\n"
    "s_cmp_lt_u32 s100, 780\n"
    "s_cbranch_scc1 .Lk_u70\n"
    "s_add_u32 m0, s75, 0x3000\n"
    "s_nop 0\n"
    "buffer_load_dwordx4 v36, s[48:51], s79 offen offset:0 nt lds\n"
    "s_add_u32 m0, s75, 0x3380\n"
    "s_nop 0\n"
    "buffer_load_dwordx4 v36, s[48:51], s79 offen offset:128 nt lds\n"
    "buffer_load_dwordx4 a[96:99], v36, s[48:51], s79 offen offset:256 nt\n"
    "buffer_load_dwordx4 a[100:103], v36, s[48:51], s79 offen offset:384 nt\n"
    ".Lk_u71:\n"
    "s_memrealtime s[100:101]\n"
    "s_waitcnt lgkmcnt(0)\n"
    "s_sub_u32 s100, s100, s92\n"
    "s_cmp_lt_u32 s100, 841\n"
    "s_cbranch_scc1 .Lk_u71\n"
    "buffer_load_dwordx4 a[112:115], v36, s[48:51], s79 offen offset:512 nt\n"
    "buffer_load_dwordx4 a[116:119], v36, s[48:51], s79 offen offset:640 nt\n"
    "buffer_load_dwordx4 a[120:123], v36, s[48:51], s79 offen offset:768 nt\n"
    "buffer_load_dwordx4 a[124:127], v36, s[48:51], s79 offen offset:896 nt\n"
    "v_mov_b64 v[20:21], 0\n"
    "v_mov_b64 v[22:23], 0\n"
    "v_mov_b64 v[24:25], 0\n"
    "v_mov_b64 v[26:27], 0\n"
    "v_and_b32 v28, 0x3f803f80, v2\n"
    "v_and_b32 v29, 0x3f803f80, v3\n"
    "v_and_b32 v30, 0x3f803f80, v2\n"
    "v_and_b32 v31, 0x3f803f80, v3\n"
    "s_lshl_b32 s72, s64, 3\n"
    "s_add_u32 s72, s72, 0x3fc0\n"
    "v_lshrrev_b32 v32, 7, v1\n"
    "v_add_u32 v32, s72, v32\n"
    "s_waitcnt vmcnt(32)\n"
    "s_cmp_eq_u32 s65, s66\n"
    "s_cselect_b32 s73, -1, 0\n"
    "v_and_b32 v7, s73, v7\n"
    "v_cndmask_b32 v51, 0, v35, s[98:99]\n"
    "v_mov_b32 v50, 0x3f803f80\n"
    "v_dot2_f32_bf16 v48, v51, v50, 0\n"
    "v_dot2_f32_bf16 v49, v51, v51, 0\n"
    "v_smfmac_f32_16x16x64_bf16 v[24:27], v[28:31], v[56:63], v4\n"
    "v_smfmac_f32_16x16x64_bf16 v[24:27], v[28:31], v[64:71], v4\n"
    "v_smfmac_f32_16x16x64_bf16 v[24:27], v[28:31], v[72:79], v4\n"
    "v_smfmac_f32_16x16x64_bf16 v[24:27], v[28:31], v[80:87], v4\n"
    "v_smfmac_f32_16x16x64_bf16 v[24:27], v[28:31], v[88:95], v4\n"
    "v_smfmac_f32_16x16x64_bf16 v[24:27], v[28:31], v[96:103], v4\n"
    "v_smfmac_f32_16x16x64_bf16 v[24:27], v[28:31], v[104:111], v4\n"
    "v_and_b32_dpp v40, v35, v2 row_newbcast:0 row_mask:0xf bank_mask:0xf\n"
    "v_and_b32_dpp v41, v35, v3 row_newbcast:0 row_mask:0xf bank_mask:0xf\n"
    "v_and_b32_dpp v42, v35, v2 row_newbcast:1 row_mask:0xf bank_mask:0xf\n"
    "v_and_b32_dpp v43, v35, v3 row_newbcast:1 row_mask:0xf bank_mask:0xf\n"
    "v_and_b32_dpp v44, v35, v2 row_newbcast:2 row_mask:0xf bank_mask:0xf\n"
    "v_and_b32_dpp v45, v35, v3 row_newbcast:2 row_mask:0xf bank_mask:0xf\n"
    "v_and_b32_dpp v46, v35, v2 row_newbcast:3 row_mask:0xf bank_mask:0xf\n"
    "v_and_b32_dpp v47, v35, v3 row_newbcast:3 row_mask:0xf bank_mask:0xf\n"
    "v_permlane32_swap_b32 v48, v49\n"
    "v_add_f32 v48, v48, v49\n"
    "v_smfmac_f32_16x16x64_bf16 v[20:23], v[40:43], v[56:63], v4\n"
    "v_and_b32_dpp v40, v35, v2 row_newbcast:4 row_mask:0xf bank_mask:0xf\n"
    "v_and_b32_dpp v41, v35, v3 row_newbcast:4 row_mask:0xf bank_mask:0xf\n"
    "v_and_b32_dpp v42, v35, v2 row_newbcast:5 row_mask:0xf bank_mask:0xf\n"
    "v_and_b32_dpp v43, v35, v3 row_newbcast:5 row_mask:0xf bank_mask:0xf\n"
    "v_mov_b32 v50, v48\n"
    "s_nop 1\n"
    "v_permlane16_swap_b32 v48, v50\n"
    "v_smfmac_f32_16x16x64_bf16 v[20:23], v[44:47], v[64:71], v4\n"
    "v_and_b32_dpp v44, v35, v2 row_newbcast:6 row_mask:0xf bank_mask:0xf\n"
    "v_and_b32_dpp v45, v35, v3 row_newbcast:6 row_mask:0xf bank_mask:0xf\n"
    "v_and_b32_dpp v46, v35, v2 row_newbcast:7 row_mask:0xf bank_mask:0xf\n"
    "v_and_b32_dpp v47, v35, v3 row_newbcast:7 row_mask:0xf bank_mask:0xf\n"
    "v_add_f32 v48, v48, v50\n"
    "v_smfmac_f32_16x16x64_bf16 v[20:23], v[40:43], v[72:79], v4\n"
    "v_and_b32_dpp v40, v35, v2 row_newbcast:8 row_mask:0xf bank_mask:0xf\n"
    "v_and_b32_dpp v41, v35, v3 row_newbcast:8 row_mask:0xf bank_mask:0xf\n"
    "v_and_b32_dpp v42, v35, v2 row_newbcast:9 row_mask:0xf bank_mask:0xf\n"
    "v_and_b32_dpp v43, v35, v3 row_newbcast:9 row_mask:0xf bank_mask:0xf\n"
    "v_add_f32_dpp v48, v48, v48 row_ror:8 row_mask:0xf bank_mask:0xf\n"
    "v_smfmac_f32_16x16x64_bf16 v[20:23], v[44:47], v[80:87], v4\n"
    "v_and_b32_dpp v44, v35, v2 row_newbcast:10 row_mask:0xf bank_mask:0xf\n"
    "v_and_b32_dpp v45, v35, v3 row_newbcast:10 row_mask:0xf bank_mask:0xf\n"
    "v_and_b32_dpp v46, v35, v2 row_newbcast:11 row_mask:0xf bank_mask:0xf\n"
    "v_and_b32_dpp v47, v35, v3 row_newbcast:11 row_mask:0xf bank_mask:0xf\n"
    "v_add_f32_dpp v48, v48, v48 row_ror:4 row_mask:0xf bank_mask:0xf\n"
    "v_smfmac_f32_16x16x64_bf16 v[20:23], v[40:43], v[88:95], v4\n"
    "v_and_b32_dpp v40, v35, v2 row_newbcast:12 row_mask:0xf bank_mask:0xf\n"
    "v_and_b32_dpp v41, v35, v3 row_newbcast:12 row_mask:0xf bank_mask:0xf\n"
    "v_and_b32_dpp v42, v35, v2 row_newbcast:13 row_mask:0xf bank_mask:0xf\n"
    "v_and_b32_dpp v43, v35, v3 row_newbcast:13 row_mask:0xf bank_mask:0xf\n"
    "v_add_f32_dpp v48, v48, v48 row_ror:2 row_mask:0xf bank_mask:0xf\n"
    "v_smfmac_f32_16x16x64_bf16 v[20:23], v[44:47], v[96:103], v4\n"
    "s_nop 0\n"
    "v_add_f32_dpp v48, v48, v48 row_ror:1 row_mask:0xf bank_mask:0xf\n"
    "v_smfmac_f32_16x16x64_bf16 v[20:23], v[40:43], v[104:111], v4\n"
    "s_mov_b64 exec, s[96:97]\n"
    "ds_write_b32 v32, v48\n"
    "s_mov_b64 exec, -1\n"
    "ds_write_b128 v15, v[24:27] offset:4096\n"
    "s_nop 3\n"
    "ds_write_b128 v15, v[20:23] offset:0\n"
    "s_waitcnt lgkmcnt(0)\n"
    "s_barrier\n"
    "s_cmp_lg_u32 s64, 0\n"
    "s_cbranch_scc1 .Lk_p0done\n"
    "v_mov_b32 v32, 0x3fc0\n"
    "v_add_u32 v33, 0x1000, v19\n"
    "ds_read_b128 v[56:59], v32\n"
    "ds_read_b128 v[60:63], v32 offset:16\n"
    "ds_read2_b64 v[64:67], v19 offset0:0 offset1:32\n"
    "ds_read2_b64 v[80:83], v33 offset0:0 offset1:32\n"
    "ds_read2_b64 v[68:71], v19 offset0:64 offset1:96\n"
    "ds_read2_b64 v[84:87], v33 offset0:64 offset1:96\n"
    "ds_read2_b64 v[72:75], v19 offset0:128 offset1:160\n"
    "ds_read2_b64 v[88:91], v33 offset0:128 offset1:160\n"
    "ds_read2_b64 v[76:79], v19 offset0:192 offset1:224\n"
    "ds_read2_b64 v[92:95], v33 offset0:192 offset1:224\n"
    "v_mov_b32 v96, 0\n"
    "v_mov_b32 v97, 0x3727c5ac\n"
    "v_mov_b32 v105, s68\n"
    "v_mov_b32 v107, s68\n"
    "s_mov_b32 s94, 0x3b124925\n"
    "s_mov_b32 s95, 0x3b124925\n"
    "s_waitcnt lgkmcnt(0)\n"
    "v_pk_add_f32 v[56:57], v[56:57], v[58:59]\n"
    "v_pk_add_f32 v[60:61], v[60:61], v[62:63]\n"
    "v_pk_add_f32 v[56:57], v[56:57], v[60:61]\n"
    "v_pk_fma_f32 v[56:57], v[56:57], s[94:95], v[96:97]\n"
    "v_fma_f32 v57, -v56, v56, v57\n"
    "v_max_f32 v57, v97, v57\n"
    "v_rsq_f32 v57, v57\n"
    "v_pk_add_f32 v[64:65], v[64:65], v[66:67]\n"
    "v_pk_add_f32 v[64:65], v[64:65], v[68:69]\n"
    "v_pk_add_f32 v[64:65], v[64:65], v[70:71]\n"
    "v_pk_add_f32 v[64:65], v[64:65], v[72:73]\n"
    "v_pk_add_f32 v[64:65], v[64:65], v[74:75]\n"
    "v_pk_add_f32 v[64:65], v[64:65], v[76:77]\n"
    "v_pk_add_f32 v[64:65], v[64:65], v[78:79]\n"
    "v_pk_add_f32 v[80:81], v[80:81], v[82:83]\n"
    "v_pk_add_f32 v[80:81], v[80:81], v[84:85]\n"
    "v_pk_add_f32 v[80:81], v[80:81], v[86:87]\n"
    "v_pk_add_f32 v[80:81], v[80:81], v[88:89]\n"
    "v_pk_add_f32 v[80:81], v[80:81], v[90:91]\n"
    "v_pk_add_f32 v[80:81], v[80:81], v[92:93]\n"
    "v_pk_add_f32 v[80:81], v[80:81], v[94:95]\n"
    "v_mul_f32 v57, 0x3db8aa3b, v57\n"
    "v_fma_f32 v64, -v56, v80, v64\n"
    "v_mul_f32 v64, v64, v57\n"
    "v_fma_f32 v65, -v56, v81, v65\n"
    "v_mul_f32 v65, v65, v57\n"
    "v_cvt_pk_bf16_f32 v104, v64, v65\n"
    "v_cvt_pk_bf16_f32 v106, v80, v81\n"
    "s_lshl_b32 s71, s65, 3\n"
    "s_add_u32 s71, s71, s66\n"
    "s_xor_b32 s71, s71, 4\n"
    "s_lshl_b32 s71, s71, 9\n"
    "s_mov_b32 exec_hi, 0\n"
    "buffer_store_dwordx4 v[104:107], v1, s[32:35], s71 offen sc1\n"
    "s_mov_b32 exec_hi, -1\n"
    ".Lk_p0done:\n"
    ".Lk_u72:\n"
    "s_memrealtime s[100:101]\n"
    "s_waitcnt lgkmcnt(0)\n"
    "s_sub_u32 s100, s100, s92\n"
    "s_cmp_lt_u32 s100, 902\n"
    "s_cbranch_scc1 .Lk_u72\n"
    "buffer_load_dwordx4 a[136:139], v39, s[48:51], s79 offen offset:0 nt\n"
    "buffer_load_dwordx4 a[140:143], v39, s[48:51], s79 offen offset:128 nt\n"
    "s_add_u32 m0, s75, 0x4700\n"
    "s_nop 0\n"
    "buffer_load_dwordx4 v39, s[48:51], s79 offen offset:256 nt lds\n"
    "s_add_u32 m0, s75, 0x4a80\n"
    "s_nop 0\n"
    "buffer_load_dwordx4 v39, s[48:51], s79 offen offset:384 nt lds\n"
    ".Lk_u73:\n"
    "s_memrealtime s[100:101]\n"
    "s_waitcnt lgkmcnt(0)\n"
    "s_sub_u32 s100, s100, s92\n"
    "s_cmp_lt_u32 s100, 963\n"
    "s_cbranch_scc1 .Lk_u73\n"
    "buffer_load_dwordx4 a[160:163], v39, s[48:51], s79 offen offset:512 nt\n"
    "buffer_load_dwordx4 a[164:167], v39, s[48:51], s79 offen offset:640 nt\n"
    "buffer_load_dwordx4 a[168:171], v39, s[48:51], s79 offen offset:768 nt\n"
    "buffer_load_dwordx4 a[172:175], v39, s[48:51], s79 offen offset:896 nt\n"
    "s_mul_i32 s48, s60, -6\n"
    "s_ashr_i32 s49, s48, 31\n"
    "buffer_load_dwordx4 v[56:59], v9, s[40:43], 0 offen offset:0\n"
    ".Lk_u75:\n"
    "s_memrealtime s[100:101]\n"
    "s_waitcnt lgkmcnt(0)\n"
    "s_sub_u32 s100, s100, s92\n"
    "s_cmp_lt_u32 s100, 1024\n"
    "s_cbranch_scc1 .Lk_u75\n"
    "s_add_u32 m0, s75, 0x5000\n"
    "s_nop 0\n"
    "buffer_load_dwordx4 v37, s[52:55], s76 offen offset:0 nt lds\n"
    "buffer_load_dwordx4 a[176:179], v37, s[52:55], s76 offen offset:256 nt\n"
    "s_mov_b32 s72, 0xe000\n"
    "buffer_load_dwordx4 v[60:63], v9, s[40:43], s72 offen offset:0\n"
    "s_add_u32 m0, s75, 0x5400\n"
    "s_nop 0\n"
    "buffer_load_dwordx4 v37, s[52:55], s76 offen offset:1024 nt lds\n"
    "buffer_load_dwordx4 a[184:187], v37, s[52:55], s76 offen offset:1280 nt\n"
    "buffer_load_dwordx4 v[64:67], v9, s[40:43], 0 offen offset:3584\n"
    ".Lk_u78:\n"
    "s_memrealtime s[100:101]\n"
    "s_waitcnt lgkmcnt(0)\n"
    "s_sub_u32 s100, s100, s92\n"
    "s_cmp_lt_u32 s100, 1091\n"
    "s_cbranch_scc1 .Lk_u78\n"
    "s_add_u32 m0, s75, 0x5800\n"
    "s_nop 0\n"
    "buffer_load_dwordx4 v37, s[52:55], s76 offen offset:2048 nt lds\n"
    "buffer_load_dwordx4 a[192:195], v37, s[52:55], s76 offen offset:2304 nt\n"
    "s_mov_b32 s71, 0xe000\n"
    "buffer_load_dwordx4 v[68:71], v9, s[40:43], s71 offen offset:3584\n"
    "s_add_u32 m0, s75, 0x5c00\n"
    "s_nop 0\n"
    "buffer_load_dwordx4 v37, s[52:55], s76 offen offset:3072 nt lds\n"
    "buffer_load_dwordx4 a[200:203], v37, s[52:55], s76 offen offset:3328 nt\n"
    "s_mov_b32 s72, 0x1000\n"
    "buffer_load_dwordx4 v[72:75], v9, s[40:43], s72 offen offset:3072\n"
    ".Lk_u81:\n"
    "s_memrealtime s[100:101]\n"
    "s_waitcnt lgkmcnt(0)\n"
    "s_sub_u32 s100, s100, s92\n"
    "s_cmp_lt_u32 s100, 1159\n"
    "s_cbranch_scc1 .Lk_u81\n"
    "s_add_u32 m0, s75, 0x7000\n"
    "s_nop 0\n"
    "buffer_load_dwordx4 v38, s[52:55], s76 offen offset:0 nt lds\n"
    "buffer_load_dwordx4 a[208:211], v38, s[52:55], s76 offen offset:256 nt\n"
    "s_mov_b32 s72, 0xf000\n"
    "buffer_load_dwordx4 v[76:79], v9, s[40:43], s72 offen offset:3072\n"
    "s_add_u32 m0, s75, 0x7400\n"
    "s_nop 0\n"
    "buffer_load_dwordx4 v38, s[52:55], s76 offen offset:1024 nt lds\n"
    "buffer_load_dwordx4 a[216:219], v38, s[52:55], s76 offen offset:1280 nt\n"
    "s_mov_b32 s71, 0x2000\n"
    "buffer_load_dwordx4 v[80:83], v9, s[40:43], s71 offen offset:2560\n"
    ".Lk_u84:\n"
    "s_memrealtime s[100:101]\n"
    "s_waitcnt lgkmcnt(0)\n"
    "s_sub_u32 s100, s100, s92\n"
    "s_cmp_lt_u32 s100, 1227\n"
    "s_cbranch_scc1 .Lk_u84\n"
    "s_add_u32 m0, s75, 0x7800\n"
    "s_nop 0\n"
    "buffer_load_dwordx4 v38, s[52:55], s76 offen offset:2048 nt lds\n"
    "buffer_load_dwordx4 a[224:227], v38, s[52:55], s76 offen offset:2304 nt\n"
    "s_mov_b32 s71, 0x10000\n"
    "buffer_load_dwordx4 v[84:87], v9, s[40:43], s71 offen offset:2560\n"
    "s_add_u32 m0, s75, 0x7c00\n"
    "s_nop 0\n"
    "buffer_load_dwordx4 v38, s[52:55], s76 offen offset:3072 nt lds\n"
    "buffer_load_dwordx4 a[232:235], v38, s[52:55], s76 offen offset:3328 nt\n"
    "s_mov_b32 s72, 0x3000\n"
    "buffer_load_dwordx4 v[88:91], v9, s[40:43], s72 offen offset:2048\n"
    ".Lk_u87:\n"
    "s_memrealtime s[100:101]\n"
    "s_waitcnt lgkmcnt(0)\n"
    "s_sub_u32 s100, s100, s92\n"
    "s_cmp_lt_u32 s100, 1294\n"
    "s_cbranch_scc1 .Lk_u87\n"
    "s_add_u32 m0, s75, 0x5400\n"
    "s_nop 0\n"
    "buffer_load_dwordx4 v37, s[52:55], s77 offen offset:0 nt lds\n"
    "buffer_load_dwordx4 a[180:183], v37, s[52:55], s77 offen offset:256 nt\n"
    "s_mov_b32 s72, 0x11000\n"
    "buffer_load_dwordx4 v[92:95], v9, s[40:43], s72 offen offset:2048\n"
    "s_add_u32 m0, s75, 0x5800\n"
    "s_nop 0\n"
    "buffer_load_dwordx4 v37, s[52:55], s77 offen offset:1024 nt lds\n"
    "buffer_load_dwordx4 a[188:191], v37, s[52:55], s77 offen offset:1280 nt\n"
    "s_cmp_eq_u32 s66, 7\n"
    "s_cbranch_scc1 .Lk_nopw\n"
    "v_and_b32 v29, 16, v1\n"
    "v_lshlrev_b32 v29, 3, v29\n"
    "v_bfe_u32 v30, v1, 5, 1\n"
    "v_lshl_add_u32 v31, v30, 14, v29\n"
    "buffer_load_dword v142, v31, s[32:35], s88 offen\n"
    "v_mul_u32_u24 v30, 0x3800, v30\n"
    "v_add_u32 v30, v30, v29\n"
    "buffer_load_dword v142, v30, s[32:35], s90 offen\n"
    ".Lk_nopw:\n"
    "v_and_b32 v29, 48, v1\n"
    "v_lshlrev_b32 v29, 3, v29\n"
    "buffer_load_dword v142, v29, s[32:35], s84 offen\n"
    "buffer_load_dword v142, off, s[32:35], s85\n"
    "s_mov_b32 s71, 0x4000\n"
    "buffer_load_dwordx4 v[96:99], v9, s[40:43], s71 offen offset:1536\n"
    "s_lshr_b32 s73, s82, 2\n"
    "buffer_load_dwordx4 v[40:43], v1, s[32:35], s73 offen offset:0 sc1\n"
    ".Lk_u90:\n"
    "s_memrealtime s[100:101]\n"
    "s_waitcnt lgkmcnt(0)\n"
    "s_sub_u32 s100, s100, s92\n"
    "s_cmp_lt_u32 s100, 1362\n"
    "s_cbranch_scc1 .Lk_u90\n"
    "s_add_u32 m0, s75, 0x5c00\n"
    "s_nop 0\n"
    "buffer_load_dwordx4 v37, s[52:55], s77 offen offset:2048 nt lds\n"
    "buffer_load_dwordx4 a[196:199], v37, s[52:55], s77 offen offset:2304 nt\n"
    "s_mov_b32 s71, 0x12000\n"
    "buffer_load_dwordx4 v[100:103], v9, s[40:43], s71 offen offset:1536\n"
    "s_add_u32 m0, s75, 0x6000\n"
    "s_nop 0\n"
    "buffer_load_dwordx4 v37, s[52:55], s77 offen offset:3072 nt lds\n"
    "buffer_load_dwordx4 a[204:207], v37, s[52:55], s77 offen offset:3328 nt\n"
    "s_mov_b32 s72, 0x5000\n"
    "buffer_load_dwordx4 v[104:107], v9, s[40:43], s72 offen offset:1024\n"
    ".Lk_u93:\n"
    "s_memrealtime s[100:101]\n"
    "s_waitcnt lgkmcnt(0)\n"
    "s_sub_u32 s100, s100, s92\n"
    "s_cmp_lt_u32 s100, 1429\n"
    "s_cbranch_scc1 .Lk_u93\n"
    "s_add_u32 m0, s75, 0x7400\n"
    "s_nop 0\n"
    "buffer_load_dwordx4 v38, s[52:55], s77 offen offset:0 nt lds\n"
    "buffer_load_dwordx4 a[212:215], v38, s[52:55], s77 offen offset:256 nt\n"
    "s_mov_b32 s72, 0x13000\n"
    "buffer_load_dwordx4 v[108:111], v9, s[40:43], s72 offen offset:1024\n"
    "s_add_u32 m0, s75, 0x7800\n"
    "s_nop 0\n"
    "buffer_load_dwordx4 v38, s[52:55], s77 offen offset:1024 nt lds\n"
    "buffer_load_dwordx4 a[220:223], v38, s[52:55], s77 offen offset:1280 nt\n"
    "s_mov_b32 s71, 0x6000\n"
    "buffer_load_dwordx4 v[112:115], v9, s[40:43], s71 offen offset:512\n"
    ".Lk_u96:\n"
    "s_memrealtime s[100:101]\n"
    "s_waitcnt lgkmcnt(0)\n"
    "s_sub_u32 s100, s100, s92\n"
    "s_cmp_lt_u32 s100, 1497\n"
    "s_cbranch_scc1 .Lk_u96\n"
    "s_add_u32 m0, s75, 0x7c00\n"
    "s_nop 0\n"
    "buffer_load_dwordx4 v38, s[52:55], s77 offen offset:2048 nt lds\n"
    "buffer_load_dwordx4 a[228:231], v38, s[52:55], s77 offen offset:2304 nt\n"
    "s_mov_b32 s71, 0x14000\n"
    "buffer_load_dwordx4 v[116:119], v9, s[40:43], s71 offen offset:512\n"
    "s_add_u32 m0, s75, 0x8000\n"
    "s_nop 0\n"
    "buffer_load_dwordx4 v38, s[52:55], s77 offen offset:3072 nt lds\n"
    "buffer_load_dwordx4 a[236:239], v38, s[52:55], s77 offen offset:3328 nt\n"
    ".Lk_u98:\n"
    "s_memrealtime s[100:101]\n"
    "s_waitcnt lgkmcnt(0)\n"
    "s_sub_u32 s100, s100, s92\n"
    "s_cmp_lt_u32 s100, 1565\n"
    "s_cbranch_scc1 .Lk_u98\n"
    "buffer_load_dwordx4 a[240:243], v37, s[52:55], s78 offen offset:0 nt\n"
    "buffer_load_dwordx4 a[248:251], v37, s[52:55], s78 offen offset:256 nt\n"
    "buffer_load_dwordx4 v[144:147], v37, s[52:55], s78 offen offset:1024 nt\n"
    "buffer_load_dwordx4 v[152:155], v37, s[52:55], s78 offen offset:1280 nt\n"
    ".Lk_u99:\n"
    "s_memrealtime s[100:101]\n"
    "s_waitcnt lgkmcnt(0)\n"
    "s_sub_u32 s100, s100, s92\n"
    "s_cmp_lt_u32 s100, 1626\n"
    "s_cbranch_scc1 .Lk_u99\n"
    "buffer_load_dwordx4 v[160:163], v37, s[52:55], s78 offen offset:2048 nt\n"
    "buffer_load_dwordx4 v[168:171], v37, s[52:55], s78 offen offset:2304 nt\n"
    "buffer_load_dwordx4 v[176:179], v37, s[52:55], s78 offen offset:3072 nt\n"
    "buffer_load_dwordx4 v[184:187], v37, s[52:55], s78 offen offset:3328 nt\n"
    ".Lk_u100:\n"
    "s_memrealtime s[100:101]\n"
    "s_waitcnt lgkmcnt(0)\n"
    "s_sub_u32 s100, s100, s92\n"
    "s_cmp_lt_u32 s100, 1687\n"
    "s_cbranch_scc1 .Lk_u100\n"
    "buffer_load_dwordx4 v[192:195], v38, s[52:55], s78 offen offset:0 nt\n"
    "buffer_load_dwordx4 v[200:203], v38, s[52:55], s78 offen offset:256 nt\n"
    "buffer_load_dwordx4 v[208:211], v38, s[52:55], s78 offen offset:1024 nt\n"
    "buffer_load_dwordx4 v[216:219], v38, s[52:55], s78 offen offset:1280 nt\n"
    "v_mov_b64 v[20:21], 0\n"
    "v_mov_b64 v[22:23], 0\n"
    "v_mov_b64 v[24:25], 0\n"
    "v_mov_b64 v[26:27], 0\n"
    "s_waitcnt vmcnt(29)\n"
    ".Lk_q0chk:\n"
    "v_xor_b32 v29, s68, v41\n"
    "v_bitop3_b32 v29, v43, v29, s68 bitop3:0xde\n"
    "v_cmp_ne_u32 vcc, 0, v29\n"
    "s_cbranch_vccz .Lk_q0ok\n"
    "s_add_u32 s70, s70, 1\n"
    "s_cmp_gt_u32 s70, 0x200000\n"
    "s_cbranch_scc1 .Lk_q0ok\n"
    "s_sleep 8\n"
    "s_lshr_b32 s73, s82, 2\n"
    "buffer_load_dwordx4 v[40:43], v1, s[32:35], s73 offen offset:0 sc1\n"
    "s_waitcnt vmcnt(0)\n"
    "s_branch .Lk_q0chk\n"
    ".Lk_q0ok:\n"
    "v_add_u32 v36, s75, v1\n"
    "ds_read_b128 v[120:123], v36 offset:0\n"
    "ds_read_b128 v[124:127], v36 offset:1024\n"
    "ds_read_b128 v[128:131], v36 offset:2048\n"
    "ds_read_b128 v[132:135], v36 offset:3072\n"
    "ds_read_b128 v[136:139], v36 offset:4096\n"
    "ds_read_b128 v[140:143], v36 offset:5120\n"
    "v_mov_b32 v5, v42\n"
    "v_mov_b32 v30, v40\n"
    ".Lk_attn:\n"
    "ds_write_b32 v16, v30\n"
    "v_mov_b64 v[40:41], 0\n"
    "v_mov_b64 v[42:43], 0\n"
    "v_mov_b64 v[44:45], 0\n"
    "v_mov_b64 v[46:47], 0\n"
    "v_mov_b64 v[48:49], 0\n"
    "v_mov_b64 v[50:51], 0\n"
    "v_mov_b64 v[52:53], 0\n"
    "v_mov_b64 v[54:55], 0\n"
    "s_mov_b32 vcc_lo, 0xaaaaaaaa\n"
    "s_mov_b32 vcc_hi, 0xaaaaaaaa\n"
    "s_mov_b32 exec_lo, 0xff00ff\n"
    "s_mov_b32 exec_hi, 0xff00ff\n"
    "s_waitcnt lgkmcnt(0)\n"
    "s_barrier\n"
    "ds_read_b128 v[40:43], v17 offset:0\n"
    "ds_read_b128 v[44:47], v17 offset:64\n"
    "s_not_b64 exec, exec\n"
    "ds_read_b128 v[48:51], v17 offset:0\n"
    "ds_read_b128 v[52:55], v17 offset:64\n"
    "s_mov_b64 exec, -1\n"
    "s_waitcnt lgkmcnt(3)\n"
    "v_smfmac_f32_16x16x64_bf16 v[20:23], v[40:43], v[120:127], v4 abid:1\n"
    "ds_read_b128 v[120:123], v36 offset:6144\n"
    "ds_read_b128 v[124:127], v36 offset:7168\n"
    "v_mov_b32 v28, 0\n"
    "s_waitcnt lgkmcnt(4)\n"
    "v_smfmac_f32_16x16x64_bf16 v[20:23], v[44:47], a[0:7], v4 abid:1\n"
    "v_mov_b32 v29, 0\n"
    "s_waitcnt lgkmcnt(3)\n"
    "v_smfmac_f32_16x16x64_bf16 v[20:23], v[48:51], a[8:15], v4 abid:1\n"
    "v_mov_b32 v30, 0\n"
    "s_waitcnt lgkmcnt(2)\n"
    "v_smfmac_f32_16x16x64_bf16 v[20:23], v[52:55], v[128:135], v4 abid:1\n"
    "ds_read_b128 v[128:131], v36 offset:8192\n"
    "ds_read_b128 v[132:135], v36 offset:9216\n"
    "v_mov_b32 v31, 0\n"
    "v_smfmac_f32_16x16x64_bf16 v[24:27], v[40:43], a[16:23], v4 abid:1\n"
    "v_smfmac_f32_16x16x64_bf16 v[24:27], v[44:47], a[24:31], v4 abid:1\n"
    "v_smfmac_f32_16x16x64_bf16 v[24:27], v[48:51], v[136:143], v4 abid:1\n"
    "ds_read_b128 v[136:139], v36 offset:10240\n"
    "ds_read_b128 v[140:143], v36 offset:11264\n"
    "v_add_f32 v39, v21, v23\n"
    "v_smfmac_f32_16x16x64_bf16 v[24:27], v[52:55], a[32:39], v4 abid:1\n"
    "s_nop 0\n"
    "v_add_f32_dpp v39, v39, v20 quad_perm:[1,0,3,2] row_mask:0xf bank_mask:0xf\n"
    "v_smfmac_f32_16x16x64_bf16 v[28:31], v[40:43], a[40:47], v4 abid:1\n"
    "v_add_f32 v32, v39, v22\n"
    "s_waitcnt lgkmcnt(4)\n"
    "v_smfmac_f32_16x16x64_bf16 v[28:31], v[44:47], v[120:127], v4 abid:1\n"
    "ds_read_b128 v[120:123], v36 offset:12288\n"
    "ds_read_b128 v[124:127], v36 offset:13312\n"
    "v_sub_f32 v20, v20, v32\n"
    "v_smfmac_f32_16x16x64_bf16 v[28:31], v[48:51], a[48:55], v4 abid:1\n"
    "v_add_f32 v39, v24, v26\n"
    "v_smfmac_f32_16x16x64_bf16 v[28:31], v[52:55], a[56:63], v4 abid:1\n"
    "s_nop 0\n"
    "v_add_f32_dpp v39, v39, v25 quad_perm:[1,0,3,2] row_mask:0xf bank_mask:0xf\n"
    "s_waitcnt lgkmcnt(4)\n"
    "v_smfmac_f32_16x16x64_bf16 v[20:23], v[40:43], v[128:135], v4 abid:1\n"
    "ds_read_b128 v[128:131], v36 offset:14336\n"
    "ds_read_b128 v[132:135], v36 offset:15360\n"
    "v_add_f32 v39, v39, v27\n"
    "v_smfmac_f32_16x16x64_bf16 v[20:23], v[44:47], a[64:71], v4 abid:1\n"
    "v_sub_f32 v25, v25, v39\n"
    "v_cndmask_b32 v32, v32, v39, vcc\n"
    "v_smfmac_f32_16x16x64_bf16 v[20:23], v[48:51], a[72:79], v4 abid:1\n"
    "v_add_f32 v39, v29, v31\n"
    "s_waitcnt lgkmcnt(4)\n"
    "v_smfmac_f32_16x16x64_bf16 v[20:23], v[52:55], v[136:143], v4 abid:1\n"
    "ds_read_b128 v[136:139], v36 offset:16384\n"
    "ds_read_b128 v[140:143], v36 offset:17408\n"
    "v_add_f32_dpp v39, v39, v28 quad_perm:[1,0,3,2] row_mask:0xf bank_mask:0xf\n"
    "v_smfmac_f32_16x16x64_bf16 v[24:27], v[40:43], a[80:87], v4 abid:1\n"
    "v_add_f32 v33, v39, v30\n"
    "v_smfmac_f32_16x16x64_bf16 v[24:27], v[44:47], a[88:95], v4 abid:1\n"
    "v_sub_f32 v28, v28, v33\n"
    "s_waitcnt lgkmcnt(4)\n"
    "v_smfmac_f32_16x16x64_bf16 v[24:27], v[48:51], v[120:127], v4 abid:1\n"
    "ds_read_b128 v[120:123], v36 offset:18432\n"
    "ds_read_b128 v[124:127], v36 offset:19456\n"
    "v_add_f32 v39, v20, v22\n"
    "v_smfmac_f32_16x16x64_bf16 v[24:27], v[52:55], a[96:103], v4 abid:1\n"
    "s_nop 0\n"
    "v_add_f32_dpp v39, v39, v21 quad_perm:[1,0,3,2] row_mask:0xf bank_mask:0xf\n"
    "v_smfmac_f32_16x16x64_bf16 v[28:31], v[40:43], a[104:111], v4 abid:1\n"
    "v_add_f32 v39, v39, v23\n"
    "s_waitcnt lgkmcnt(4)\n"
    "v_smfmac_f32_16x16x64_bf16 v[28:31], v[44:47], v[128:135], v4 abid:1\n"
    "s_waitcnt vmcnt(35)\n"
    "ds_read_b128 v[128:131], v36 offset:20480\n"
    "ds_read_b128 v[132:135], v36 offset:21504\n"
    "v_sub_f32 v21, v21, v39\n"
    "v_cndmask_b32 v33, v33, v39, vcc\n"
    "v_smfmac_f32_16x16x64_bf16 v[28:31], v[48:51], a[112:119], v4 abid:1\n"
    "v_add_f32 v39, v25, v27\n"
    "v_smfmac_f32_16x16x64_bf16 v[28:31], v[52:55], a[120:127], v4 abid:1\n"
    "s_nop 0\n"
    "v_add_f32_dpp v39, v39, v24 quad_perm:[1,0,3,2] row_mask:0xf bank_mask:0xf\n"
    "s_waitcnt lgkmcnt(4)\n"
    "v_smfmac_f32_16x16x64_bf16 v[20:23], v[40:43], v[136:143], v4 abid:1\n"
    "s_waitcnt vmcnt(32)\n"
    "ds_read_b128 v[136:139], v36 offset:22528\n"
    "ds_read_b128 v[140:143], v36 offset:23552\n"
    "v_add_f32 v34, v39, v26\n"
    "v_smfmac_f32_16x16x64_bf16 v[20:23], v[44:47], a[128:135], v4 abid:1\n"
    "v_sub_f32 v24, v24, v34\n"
    "v_smfmac_f32_16x16x64_bf16 v[20:23], v[48:51], a[136:143], v4 abid:1\n"
    "v_add_f32 v39, v28, v30\n"
    "s_waitcnt lgkmcnt(4)\n"
    "v_smfmac_f32_16x16x64_bf16 v[20:23], v[52:55], v[120:127], v4 abid:1\n"
    "s_waitcnt vmcnt(28)\n"
    "ds_read_b128 v[120:123], v36 offset:24576\n"
    "ds_read_b128 v[124:127], v36 offset:25600\n"
    "v_add_f32_dpp v39, v39, v29 quad_perm:[1,0,3,2] row_mask:0xf bank_mask:0xf\n"
    "v_smfmac_f32_16x16x64_bf16 v[24:27], v[40:43], a[144:151], v4 abid:1\n"
    "v_add_f32 v39, v39, v31\n"
    "v_smfmac_f32_16x16x64_bf16 v[24:27], v[44:47], a[152:159], v4 abid:1\n"
    "v_cndmask_b32 v34, v34, v39, vcc\n"
    "v_smfmac_f32_16x16x64_bf16 v[24:27], v[48:51], a[160:167], v4 abid:1\n"
    "v_add_f32 v39, v21, v23\n"
    "v_smfmac_f32_16x16x64_bf16 v[24:27], v[52:55], a[168:175], v4 abid:1\n"
    "s_nop 0\n"
    "v_add_f32_dpp v39, v39, v20 quad_perm:[1,0,3,2] row_mask:0xf bank_mask:0xf\n"
    "v_max3_f32 v29, v32, v33, v34\n"
    "v_add_f32 v35, v39, v22\n"
    "v_mov_b64 v[40:41], 0\n"
    "v_mov_b64 v[42:43], 0\n"
    "v_mov_b64 v[44:45], 0\n"
    "v_mov_b64 v[46:47], 0\n"
    "global_load_dwordx4 v[48:51], v[10:11], off offset:-3584\n"
    "global_load_dwordx4 v[52:55], v[10:11], off offset:3584\n"
    "v_lshl_add_u64 v[10:11], v[10:11], 0, s[60:61]\n"
    "s_cmp_lg_u32 s69, 0\n"
    "s_cbranch_scc1 .Lk_u101\n"
    ".Lk_u102:\n"
    "s_memrealtime s[100:101]\n"
    "s_waitcnt lgkmcnt(0)\n"
    "s_sub_u32 s100, s100, s92\n"
    "s_cmp_lt_u32 s100, 1747\n"
    "s_cbranch_scc1 .Lk_u102\n"
    "buffer_load_dwordx4 v[224:227], v38, s[52:55], s78 offen offset:2048 nt\n"
    "buffer_load_dwordx4 v[232:235], v38, s[52:55], s78 offen offset:2304 nt\n"
    "buffer_load_dwordx4 v[240:243], v38, s[52:55], s78 offen offset:3072 nt\n"
    "buffer_load_dwordx4 v[248:251], v38, s[52:55], s78 offen offset:3328 nt\n"
    ".Lk_u103:\n"
    "s_memrealtime s[100:101]\n"
    "s_waitcnt lgkmcnt(0)\n"
    "s_sub_u32 s100, s100, s92\n"
    "s_cmp_lt_u32 s100, 1808\n"
    "s_cbranch_scc1 .Lk_u103\n"
    "buffer_load_dwordx4 a[244:247], v37, s[52:55], s79 offen offset:0 nt\n"
    "buffer_load_dwordx4 a[252:255], v37, s[52:55], s79 offen offset:256 nt\n"
    "buffer_load_dwordx4 v[148:151], v37, s[52:55], s79 offen offset:1024 nt\n"
    "buffer_load_dwordx4 v[156:159], v37, s[52:55], s79 offen offset:1280 nt\n"
    ".Lk_u104:\n"
    "s_memrealtime s[100:101]\n"
    "s_waitcnt lgkmcnt(0)\n"
    "s_sub_u32 s100, s100, s92\n"
    "s_cmp_lt_u32 s100, 1869\n"
    "s_cbranch_scc1 .Lk_u104\n"
    "buffer_load_dwordx4 v[164:167], v37, s[52:55], s79 offen offset:2048 nt\n"
    "buffer_load_dwordx4 v[172:175], v37, s[52:55], s79 offen offset:2304 nt\n"
    "buffer_load_dwordx4 v[180:183], v37, s[52:55], s79 offen offset:3072 nt\n"
    "buffer_load_dwordx4 v[188:191], v37, s[52:55], s79 offen offset:3328 nt\n"
    ".Lk_u101:\n"
    "v_add_f32 v39, v24, v26\n"
    "s_nop 1\n"
    "v_add_f32_dpp v39, v39, v25 quad_perm:[1,0,3,2] row_mask:0xf bank_mask:0xf\n"
    "v_add_f32 v39, v39, v27\n"
    "v_cndmask_b32 v35, v35, v39, vcc\n"
    "v_max_f32 v30, v29, v35\n"
    "v_max_f32 v31, v29, v35\n"
    "s_lshl_b32 s93, s64, 2\n"
    "s_add_u32 s93, s93, 0x3f80\n"
    "v_permlane32_swap_b32 v30, v31\n"
    "v_max_f32 v30, v30, v31\n"
    "s_nop 1\n"
    "v_max_f32_dpp v30, v30, v30 row_ror:8 row_mask:0xf bank_mask:0xf\n"
    "s_nop 1\n"
    "v_max_f32_dpp v30, v30, v30 row_ror:4 row_mask:0xf bank_mask:0xf\n"
    "s_nop 1\n"
    "v_max_f32_dpp v30, v30, v30 row_ror:2 row_mask:0xf bank_mask:0xf\n"
    "s_nop 1\n"
    "v_max_f32_dpp v30, v30, v30 row_ror:1 row_mask:0xf bank_mask:0xf\n"
    "v_sub_f32 v32, v32, v30\n"
    "v_sub_f32 v33, v33, v30\n"
    "v_sub_f32 v34, v34, v30\n"
    "v_sub_f32 v35, v35, v30\n"
    "v_exp_f32 v32, v32\n"
    "v_exp_f32 v33, v33\n"
    "v_exp_f32 v34, v34\n"
    "v_exp_f32 v35, v35\n"
    "v_permlane16_swap_b32 v32, v33\n"
    "s_nop 0\n"
    "v_permlane16_swap_b32 v34, v35\n"
    "v_cvt_pk_bf16_f32 v28, v32, v33\n"
    "v_cvt_pk_bf16_f32 v29, v34, v35\n"
    "v_pk_add_f32 v[32:33], v[32:33], v[34:35]\n"
    "v_and_b32_dpp v20, v28, v2 row_newbcast:0 row_mask:0xf bank_mask:0xf\n"
    "v_and_b32_dpp v21, v28, v3 row_newbcast:0 row_mask:0xf bank_mask:0xf\n"
    "v_and_b32_dpp v22, v28, v2 row_newbcast:1 row_mask:0xf bank_mask:0xf\n"
    "v_and_b32_dpp v23, v28, v3 row_newbcast:1 row_mask:0xf bank_mask:0xf\n"
    "v_and_b32_dpp v24, v28, v2 row_newbcast:2 row_mask:0xf bank_mask:0xf\n"
    "v_and_b32_dpp v25, v28, v3 row_newbcast:2 row_mask:0xf bank_mask:0xf\n"
    "s_waitcnt lgkmcnt(4)\n"
    "v_smfmac_f32_16x16x64_bf16 v[40:43], v[20:23], v[128:135], v4\n"
    "s_waitcnt vmcnt(39)\n"
    "ds_read_b128 v[128:131], v36 offset:26624\n"
    "ds_read_b128 v[132:135], v36 offset:27648\n"
    "v_and_b32_dpp v26, v28, v2 row_newbcast:3 row_mask:0xf bank_mask:0xf\n"
    "v_and_b32_dpp v27, v28, v3 row_newbcast:3 row_mask:0xf bank_mask:0xf\n"
    "v_smfmac_f32_16x16x64_bf16 v[44:47], v[20:23], a[176:183], v4\n"
    "v_and_b32_dpp v20, v28, v2 row_newbcast:4 row_mask:0xf bank_mask:0xf\n"
    "v_and_b32_dpp v21, v28, v3 row_newbcast:4 row_mask:0xf bank_mask:0xf\n"
    "v_permlane32_swap_b32 v32, v33\n"
    "s_waitcnt lgkmcnt(4)\n"
    "v_smfmac_f32_16x16x64_bf16 v[40:43], v[24:27], v[136:143], v4\n"
    "s_waitcnt vmcnt(36)\n"
    "ds_read_b128 v[136:139], v36 offset:28672\n"
    "ds_read_b128 v[140:143], v36 offset:29696\n"
    "v_and_b32_dpp v22, v28, v2 row_newbcast:5 row_mask:0xf bank_mask:0xf\n"
    "v_and_b32_dpp v23, v28, v3 row_newbcast:5 row_mask:0xf bank_mask:0xf\n"
    "v_smfmac_f32_16x16x64_bf16 v[44:47], v[24:27], a[184:191], v4\n"
    "v_and_b32_dpp v24, v28, v2 row_newbcast:6 row_mask:0xf bank_mask:0xf\n"
    "v_and_b32_dpp v25, v28, v3 row_newbcast:6 row_mask:0xf bank_mask:0xf\n"
    "v_add_f32 v32, v32, v33\n"
    "s_waitcnt lgkmcnt(4)\n"
    "v_smfmac_f32_16x16x64_bf16 v[40:43], v[20:23], v[120:127], v4\n"
    "s_waitcnt vmcnt(33)\n"
    "ds_read_b128 v[120:123], v36 offset:30720\n"
    "ds_read_b128 v[124:127], v36 offset:31744\n"
    "v_and_b32_dpp v26, v28, v2 row_newbcast:7 row_mask:0xf bank_mask:0xf\n"
    "v_and_b32_dpp v27, v28, v3 row_newbcast:7 row_mask:0xf bank_mask:0xf\n"
    "v_smfmac_f32_16x16x64_bf16 v[44:47], v[20:23], a[192:199], v4\n"
    "v_and_b32_dpp v20, v28, v2 row_newbcast:8 row_mask:0xf bank_mask:0xf\n"
    "v_and_b32_dpp v21, v28, v3 row_newbcast:8 row_mask:0xf bank_mask:0xf\n"
    "v_mov_b32 v31, v32\n"
    "s_waitcnt lgkmcnt(4)\n"
    "v_smfmac_f32_16x16x64_bf16 v[40:43], v[24:27], v[128:135], v4\n"
    "s_waitcnt vmcnt(30)\n"
    "ds_read_b128 v[128:131], v36 offset:32768\n"
    "ds_read_b128 v[132:135], v36 offset:33792\n"
    "v_and_b32_dpp v22, v28, v2 row_newbcast:9 row_mask:0xf bank_mask:0xf\n"
    "v_and_b32_dpp v23, v28, v3 row_newbcast:9 row_mask:0xf bank_mask:0xf\n"
    "v_smfmac_f32_16x16x64_bf16 v[44:47], v[24:27], a[200:207], v4\n"
    "v_and_b32_dpp v24, v28, v2 row_newbcast:10 row_mask:0xf bank_mask:0xf\n"
    "v_and_b32_dpp v25, v28, v3 row_newbcast:10 row_mask:0xf bank_mask:0xf\n"
    "v_permlane16_swap_b32 v32, v31\n"
    "s_waitcnt lgkmcnt(4)\n"
    "v_smfmac_f32_16x16x64_bf16 v[40:43], v[20:23], v[136:143], v4\n"
    "s_waitcnt vmcnt(27)\n"
    "ds_read_b128 v[136:139], v36 offset:34816\n"
    "ds_read_b128 v[140:143], v36 offset:35840\n"
    "v_and_b32_dpp v26, v28, v2 row_newbcast:11 row_mask:0xf bank_mask:0xf\n"
    "v_and_b32_dpp v27, v28, v3 row_newbcast:11 row_mask:0xf bank_mask:0xf\n"
    "v_smfmac_f32_16x16x64_bf16 v[44:47], v[20:23], a[208:215], v4\n"
    "v_and_b32_dpp v20, v28, v2 row_newbcast:12 row_mask:0xf bank_mask:0xf\n"
    "v_and_b32_dpp v21, v28, v3 row_newbcast:12 row_mask:0xf bank_mask:0xf\n"
    "v_add_f32 v32, v32, v31\n"
    "s_waitcnt lgkmcnt(4)\n"
    "v_smfmac_f32_16x16x64_bf16 v[40:43], v[24:27], v[120:127], v4\n"
    "v_and_b32_dpp v22, v28, v2 row_newbcast:13 row_mask:0xf bank_mask:0xf\n"
    "v_and_b32_dpp v23, v28, v3 row_newbcast:13 row_mask:0xf bank_mask:0xf\n"
    "v_smfmac_f32_16x16x64_bf16 v[44:47], v[24:27], a[216:223], v4\n"
    "v_and_b32_dpp v24, v28, v2 row_newbcast:14 row_mask:0xf bank_mask:0xf\n"
    "v_and_b32_dpp v25, v28, v3 row_newbcast:14 row_mask:0xf bank_mask:0xf\n"
    "v_add_f32_dpp v32, v32, v32 row_ror:8 row_mask:0xf bank_mask:0xf\n"
    "s_waitcnt lgkmcnt(2)\n"
    "v_smfmac_f32_16x16x64_bf16 v[40:43], v[20:23], v[128:135], v4\n"
    "v_and_b32_dpp v26, v28, v2 row_newbcast:15 row_mask:0xf bank_mask:0xf\n"
    "v_and_b32_dpp v27, v28, v3 row_newbcast:15 row_mask:0xf bank_mask:0xf\n"
    "v_smfmac_f32_16x16x64_bf16 v[44:47], v[20:23], a[224:231], v4\n"
    "v_and_b32_dpp v20, v29, v2 row_newbcast:0 row_mask:0xf bank_mask:0xf\n"
    "v_and_b32_dpp v21, v29, v3 row_newbcast:0 row_mask:0xf bank_mask:0xf\n"
    "v_add_f32_dpp v32, v32, v32 row_ror:4 row_mask:0xf bank_mask:0xf\n"
    "s_waitcnt lgkmcnt(0)\n"
    "v_smfmac_f32_16x16x64_bf16 v[40:43], v[24:27], v[136:143], v4\n"
    "v_and_b32_dpp v22, v29, v2 row_newbcast:1 row_mask:0xf bank_mask:0xf\n"
    "v_and_b32_dpp v23, v29, v3 row_newbcast:1 row_mask:0xf bank_mask:0xf\n"
    "s_waitcnt vmcnt(26)\n"
    "v_smfmac_f32_16x16x64_bf16 v[44:47], v[24:27], a[232:239], v4\n"
    "s_cmp_lg_u32 s69, 0\n"
    "s_cbranch_scc1 .Lk_u105\n"
    ".Lk_u106:\n"
    "s_memrealtime s[100:101]\n"
    "s_waitcnt lgkmcnt(0)\n"
    "s_sub_u32 s100, s100, s92\n"
    "s_cmp_lt_u32 s100, 1930\n"
    "s_cbranch_scc1 .Lk_u106\n"
    "buffer_load_dwordx4 v[196:199], v38, s[52:55], s79 offen offset:0 nt\n"
    "buffer_load_dwordx4 v[204:207], v38, s[52:55], s79 offen offset:256 nt\n"
    "buffer_load_dwordx4 v[212:215], v38, s[52:55], s79 offen offset:1024 nt\n"
    "buffer_load_dwordx4 v[220:223], v38, s[52:55], s79 offen offset:1280 nt\n"
    ".Lk_u107:\n"
    "s_memrealtime s[100:101]\n"
    "s_waitcnt lgkmcnt(0)\n"
    "s_sub_u32 s100, s100, s92\n"
    "s_cmp_lt_u32 s100, 1991\n"
    "s_cbranch_scc1 .Lk_u107\n"
    "buffer_load_dwordx4 v[228:231], v38, s[52:55], s79 offen offset:2048 nt\n"
    "buffer_load_dwordx4 v[236:239], v38, s[52:55], s79 offen offset:2304 nt\n"
    "buffer_load_dwordx4 v[244:247], v38, s[52:55], s79 offen offset:3072 nt\n"
    "buffer_load_dwordx4 v[252:255], v38, s[52:55], s79 offen offset:3328 nt\n"
    ".Lk_u105:\n"
    "v_and_b32_dpp v24, v29, v2 row_newbcast:2 row_mask:0xf bank_mask:0xf\n"
    "v_and_b32_dpp v25, v29, v3 row_newbcast:2 row_mask:0xf bank_mask:0xf\n"
    "v_add_f32_dpp v32, v32, v32 row_ror:2 row_mask:0xf bank_mask:0xf\n"
    "s_waitcnt vmcnt(15)\n"
    "v_smfmac_f32_16x16x64_bf16 v[40:43], v[20:23], a[240:247], v4\n"
    "v_and_b32_dpp v26, v29, v2 row_newbcast:3 row_mask:0xf bank_mask:0xf\n"
    "v_and_b32_dpp v27, v29, v3 row_newbcast:3 row_mask:0xf bank_mask:0xf\n"
    "s_waitcnt vmcnt(14)\n"
    "v_smfmac_f32_16x16x64_bf16 v[44:47], v[20:23], a[248:255], v4\n"
    "v_and_b32_dpp v20, v29, v2 row_newbcast:4 row_mask:0xf bank_mask:0xf\n"
    "v_and_b32_dpp v21, v29, v3 row_newbcast:4 row_mask:0xf bank_mask:0xf\n"
    "v_add_f32_dpp v32, v32, v32 row_ror:1 row_mask:0xf bank_mask:0xf\n"
    "s_waitcnt vmcnt(13)\n"
    "v_smfmac_f32_16x16x64_bf16 v[40:43], v[24:27], v[144:151], v4\n"
    "v_and_b32_dpp v22, v29, v2 row_newbcast:5 row_mask:0xf bank_mask:0xf\n"
    "v_and_b32_dpp v23, v29, v3 row_newbcast:5 row_mask:0xf bank_mask:0xf\n"
    "s_waitcnt vmcnt(12)\n"
    "v_smfmac_f32_16x16x64_bf16 v[44:47], v[24:27], v[152:159], v4\n"
    "v_and_b32_dpp v24, v29, v2 row_newbcast:6 row_mask:0xf bank_mask:0xf\n"
    "v_and_b32_dpp v25, v29, v3 row_newbcast:6 row_mask:0xf bank_mask:0xf\n"
    "v_bfe_u32 v34, v1, 9, 1\n"
    "s_waitcnt vmcnt(11)\n"
    "v_smfmac_f32_16x16x64_bf16 v[40:43], v[20:23], v[160:167], v4\n"
    "v_and_b32_dpp v26, v29, v2 row_newbcast:7 row_mask:0xf bank_mask:0xf\n"
    "v_and_b32_dpp v27, v29, v3 row_newbcast:7 row_mask:0xf bank_mask:0xf\n"
    "s_waitcnt vmcnt(10)\n"
    "v_smfmac_f32_16x16x64_bf16 v[44:47], v[20:23], v[168:175], v4\n"
    "v_and_b32_dpp v20, v29, v2 row_newbcast:8 row_mask:0xf bank_mask:0xf\n"
    "v_and_b32_dpp v21, v29, v3 row_newbcast:8 row_mask:0xf bank_mask:0xf\n"
    "v_lshl_add_u32 v34, v34, 5, s93\n"
    "s_waitcnt vmcnt(9)\n"
    "v_smfmac_f32_16x16x64_bf16 v[40:43], v[24:27], v[176:183], v4\n"
    "v_and_b32_dpp v22, v29, v2 row_newbcast:9 row_mask:0xf bank_mask:0xf\n"
    "v_and_b32_dpp v23, v29, v3 row_newbcast:9 row_mask:0xf bank_mask:0xf\n"
    "s_waitcnt vmcnt(8)\n"
    "v_smfmac_f32_16x16x64_bf16 v[44:47], v[24:27], v[184:191], v4\n"
    "v_and_b32_dpp v24, v29, v2 row_newbcast:10 row_mask:0xf bank_mask:0xf\n"
    "v_and_b32_dpp v25, v29, v3 row_newbcast:10 row_mask:0xf bank_mask:0xf\n"
    "s_waitcnt vmcnt(7)\n"
    "v_smfmac_f32_16x16x64_bf16 v[40:43], v[20:23], v[192:199], v4\n"
    "v_and_b32_dpp v26, v29, v2 row_newbcast:11 row_mask:0xf bank_mask:0xf\n"
    "v_and_b32_dpp v27, v29, v3 row_newbcast:11 row_mask:0xf bank_mask:0xf\n"
    "s_waitcnt vmcnt(6)\n"
    "v_smfmac_f32_16x16x64_bf16 v[44:47], v[20:23], v[200:207], v4\n"
    "v_and_b32_dpp v20, v29, v2 row_newbcast:12 row_mask:0xf bank_mask:0xf\n"
    "v_and_b32_dpp v21, v29, v3 row_newbcast:12 row_mask:0xf bank_mask:0xf\n"
    "s_waitcnt vmcnt(5)\n"
    "v_smfmac_f32_16x16x64_bf16 v[40:43], v[24:27], v[208:215], v4\n"
    "v_and_b32_dpp v22, v29, v2 row_newbcast:13 row_mask:0xf bank_mask:0xf\n"
    "v_and_b32_dpp v23, v29, v3 row_newbcast:13 row_mask:0xf bank_mask:0xf\n"
    "s_waitcnt vmcnt(4)\n"
    "v_smfmac_f32_16x16x64_bf16 v[44:47], v[24:27], v[216:223], v4\n"
    "v_and_b32_dpp v24, v29, v2 row_newbcast:14 row_mask:0xf bank_mask:0xf\n"
    "v_and_b32_dpp v25, v29, v3 row_newbcast:14 row_mask:0xf bank_mask:0xf\n"
    "s_waitcnt vmcnt(3)\n"
    "v_smfmac_f32_16x16x64_bf16 v[40:43], v[20:23], v[224:231], v4\n"
    "v_and_b32_dpp v26, v29, v2 row_newbcast:15 row_mask:0xf bank_mask:0xf\n"
    "v_and_b32_dpp v27, v29, v3 row_newbcast:15 row_mask:0xf bank_mask:0xf\n"
    "s_waitcnt vmcnt(2)\n"
    "v_smfmac_f32_16x16x64_bf16 v[44:47], v[20:23], v[232:239], v4\n"
    "s_waitcnt vmcnt(1)\n"
    "v_smfmac_f32_16x16x64_bf16 v[40:43], v[24:27], v[240:247], v4\n"
    "s_waitcnt vmcnt(0)\n"
    "v_smfmac_f32_16x16x64_bf16 v[44:47], v[24:27], v[248:255], v4\n"
    "s_mov_b32 exec_lo, 1\n"
    "s_mov_b32 exec_hi, 0x10000\n"
    "ds_write2_b32 v34, v30, v32 offset0:0 offset1:4\n"
    "s_mov_b64 exec, -1\n"
    "s_lshl_b32 s71, s64, 9\n"
    "s_add_u32 s71, s71, 0x1000\n"
    "v_add_u32 v36, s71, v19\n"
    "ds_write_b128 v18, v[40:43]\n"
    "ds_write_b128 v18, v[44:47] offset:512\n"
    "s_lshr_b32 s71, s64, 1\n"
    "s_lshl_b32 s71, s71, 5\n"
    "s_add_u32 s71, s71, 0x3f80\n"
    "v_mov_b32 v39, s71\n"
    "v_mov_b32 v29, s68\n"
    "v_mov_b32 v33, s68\n"
    "v_mov_b32 v35, s68\n"
    "s_waitcnt lgkmcnt(0)\n"
    "s_barrier\n"
    "ds_read_b128 v[20:23], v39\n"
    "ds_read2st64_b64 v[40:43], v36 offset0:0 offset1:4\n"
    "ds_read2st64_b64 v[44:47], v36 offset0:8 offset1:12\n"
    "ds_read_b128 v[24:27], v39 offset:16\n"
    "s_waitcnt lgkmcnt(3)\n"
    "v_max3_f32 v32, v20, v21, v22\n"
    "v_max_f32 v32, v32, v23\n"
    "v_sub_f32 v20, v20, v32\n"
    "v_sub_f32 v21, v21, v32\n"
    "v_sub_f32 v22, v22, v32\n"
    "v_sub_f32 v23, v23, v32\n"
    "v_exp_f32 v20, v20\n"
    "v_exp_f32 v21, v21\n"
    "v_exp_f32 v22, v22\n"
    "v_exp_f32 v23, v23\n"
    "s_waitcnt lgkmcnt(1)\n"
    "v_pk_mul_f32 v[30:31], v[40:41], v[20:21] op_sel_hi:[1,0]\n"
    "v_pk_fma_f32 v[30:31], v[42:43], v[20:21], v[30:31] op_sel:[0,1,0] op_sel_hi:[1,1,1]\n"
    "v_pk_fma_f32 v[30:31], v[44:45], v[22:23], v[30:31] op_sel_hi:[1,0,1]\n"
    "v_pk_fma_f32 v[30:31], v[46:47], v[22:23], v[30:31] op_sel:[0,1,0] op_sel_hi:[1,1,1]\n"
    "v_cvt_pk_bf16_f32 v28, v30, v31\n"
    "buffer_store_dwordx2 v[28:29], v19, s[32:35], s84 offen sc1\n"
    "s_waitcnt lgkmcnt(0)\n"
    "v_pk_mul_f32 v[24:25], v[24:25], v[20:21]\n"
    "v_pk_fma_f32 v[24:25], v[26:27], v[22:23], v[24:25]\n"
    "s_mov_b64 exec, 0xff\n"
    "v_add_f32 v34, v24, v25\n"
    "buffer_store_dwordx4 v[32:35], v1, s[32:35], s85 offen sc1\n"
    "s_mov_b64 exec, -1\n"
    "s_cmp_eq_u32 s66, 7\n"
    "s_cbranch_scc1 .Lk_s7\n"
    "global_load_dwordx4 v[120:123], v[10:11], off offset:-3584\n"
    "global_load_dwordx4 v[124:127], v[10:11], off offset:3584\n"
    "v_lshl_add_u64 v[10:11], v[10:11], 0, s[60:61]\n"
    "global_load_dwordx4 v[128:131], v[10:11], off offset:-3584\n"
    "global_load_dwordx4 v[132:135], v[10:11], off offset:3584\n"
    "v_lshl_add_u64 v[10:11], v[10:11], 0, s[60:61]\n"
    "global_load_dwordx4 v[136:139], v[10:11], off offset:-3584\n"
    "global_load_dwordx4 v[140:143], v[10:11], off offset:3584\n"
    "v_lshl_add_u64 v[10:11], v[10:11], 0, s[60:61]\n"
    "v_lshlrev_b32 v46, 16, v7\n"
    "v_and_b32 v47, 0xffff0000, v7\n"
    "s_sleep 4\n"
    "v_and_b32 v45, 0x70, v1\n"
    "v_lshlrev_b32 v45, 3, v45\n"
    "buffer_load_dwordx4 v[36:39], v45, s[32:35], s87 offen offset:0 sc1\n"
    "buffer_load_dwordx2 v[20:21], v19, s[32:35], s86 offen offset:0 sc1\n"
    "buffer_load_dwordx2 v[22:23], v19, s[32:35], s86 offen offset:512 sc1\n"
    "buffer_load_dwordx2 v[24:25], v19, s[32:35], s86 offen offset:1024 sc1\n"
    "buffer_load_dwordx2 v[26:27], v19, s[32:35], s86 offen offset:1536 sc1\n"
    "buffer_load_dwordx2 v[28:29], v19, s[32:35], s86 offen offset:2048 sc1\n"
    "buffer_load_dwordx2 v[30:31], v19, s[32:35], s86 offen offset:2560 sc1\n"
    "buffer_load_dwordx2 v[32:33], v19, s[32:35], s86 offen offset:3072 sc1\n"
    "buffer_load_dwordx2 v[34:35], v19, s[32:35], s86 offen offset:3584 sc1\n"
    ".Lk_a_use:\n"
    "v_mov_b64 v[42:43], 0\n"
    "s_waitcnt vmcnt(8)\n"
    "v_max_f32_dpp v41, v36, v36 row_half_mirror row_mask:0xf bank_mask:0xf\n"
    "v_xor_b32 v40, s68, v37\n"
    "v_bitop3_b32 v40, v39, v40, s68 bitop3:0xde\n"
    "v_max_f32_dpp v41, v41, v41 quad_perm:[2,3,0,1] row_mask:0xf bank_mask:0xf\n"
    "s_nop 1\n"
    "v_max_f32_dpp v41, v41, v41 quad_perm:[1,0,3,2] row_mask:0xf bank_mask:0xf\n"
    "v_sub_f32 v41, v36, v41\n"
    "v_exp_f32 v41, v41\n"
    "s_nop 0\n"
    "v_cvt_pk_bf16_f32 v41, v41, 0\n"
    "v_lshlrev_b32 v44, 16, v41\n"
    "v_mul_f32 v45, v44, v38\n"
    "s_waitcnt vmcnt(7)\n"
    "v_dot2c_f32_bf16_dpp v42, v41, v20 row_newbcast:0 row_mask:0xf bank_mask:0xf\n"
    "v_dot2c_f32_bf16_dpp v43, v44, v20 row_newbcast:0 row_mask:0xf bank_mask:0xf\n"
    "v_bitop3_b32 v40, v21, v40, s68 bitop3:0xde\n"
    "v_add_f32_dpp v45, v45, v45 row_half_mirror row_mask:0xf bank_mask:0xf\n"
    "s_waitcnt vmcnt(6)\n"
    "v_dot2c_f32_bf16_dpp v42, v41, v22 row_newbcast:1 row_mask:0xf bank_mask:0xf\n"
    "v_dot2c_f32_bf16_dpp v43, v44, v22 row_newbcast:1 row_mask:0xf bank_mask:0xf\n"
    "v_bitop3_b32 v40, v23, v40, s68 bitop3:0xde\n"
    "v_add_f32_dpp v45, v45, v45 quad_perm:[2,3,0,1] row_mask:0xf bank_mask:0xf\n"
    "s_waitcnt vmcnt(5)\n"
    "v_dot2c_f32_bf16_dpp v42, v41, v24 row_newbcast:2 row_mask:0xf bank_mask:0xf\n"
    "v_dot2c_f32_bf16_dpp v43, v44, v24 row_newbcast:2 row_mask:0xf bank_mask:0xf\n"
    "v_bitop3_b32 v40, v25, v40, s68 bitop3:0xde\n"
    "v_add_f32_dpp v45, v45, v45 quad_perm:[1,0,3,2] row_mask:0xf bank_mask:0xf\n"
    "s_waitcnt vmcnt(4)\n"
    "v_dot2c_f32_bf16_dpp v42, v41, v26 row_newbcast:3 row_mask:0xf bank_mask:0xf\n"
    "v_dot2c_f32_bf16_dpp v43, v44, v26 row_newbcast:3 row_mask:0xf bank_mask:0xf\n"
    "v_bitop3_b32 v40, v27, v40, s68 bitop3:0xde\n"
    "v_rcp_f32 v45, v45\n"
    "s_waitcnt vmcnt(3)\n"
    "v_dot2c_f32_bf16_dpp v42, v41, v28 row_newbcast:4 row_mask:0xf bank_mask:0xf\n"
    "v_dot2c_f32_bf16_dpp v43, v44, v28 row_newbcast:4 row_mask:0xf bank_mask:0xf\n"
    "v_bitop3_b32 v40, v29, v40, s68 bitop3:0xde\n"
    "s_waitcnt vmcnt(2)\n"
    "v_dot2c_f32_bf16_dpp v42, v41, v30 row_newbcast:5 row_mask:0xf bank_mask:0xf\n"
    "v_dot2c_f32_bf16_dpp v43, v44, v30 row_newbcast:5 row_mask:0xf bank_mask:0xf\n"
    "v_bitop3_b32 v40, v31, v40, s68 bitop3:0xde\n"
    "s_waitcnt vmcnt(1)\n"
    "v_dot2c_f32_bf16_dpp v42, v41, v32 row_newbcast:6 row_mask:0xf bank_mask:0xf\n"
    "v_dot2c_f32_bf16_dpp v43, v44, v32 row_newbcast:6 row_mask:0xf bank_mask:0xf\n"
    "v_bitop3_b32 v40, v33, v40, s68 bitop3:0xde\n"
    "s_waitcnt vmcnt(0)\n"
    "v_dot2c_f32_bf16_dpp v42, v41, v34 row_newbcast:7 row_mask:0xf bank_mask:0xf\n"
    "v_dot2c_f32_bf16_dpp v43, v44, v34 row_newbcast:7 row_mask:0xf bank_mask:0xf\n"
    "v_bitop3_b32 v40, v35, v40, s68 bitop3:0xde\n"
    "v_cmp_ne_u32 vcc, 0, v40\n"
    "s_cbranch_vccnz .Lk_a_retry\n"
    ".Lk_a_ok:\n"
    "v_pk_mul_f32 v[42:43], v[42:43], v[44:45] op_sel:[0,1] op_sel_hi:[1,1]\n"
    "v_cvt_pk_bf16_f32 v42, v42, v43\n"
    "v_mov_b64 v[36:37], 0\n"
    "v_mov_b64 v[38:39], 0\n"
    "v_and_b32_dpp v20, v42, v2 row_newbcast:0 row_mask:0xf bank_mask:0xf\n"
    "v_and_b32_dpp v21, v42, v3 row_newbcast:0 row_mask:0xf bank_mask:0xf\n"
    "v_and_b32_dpp v22, v42, v2 row_newbcast:1 row_mask:0xf bank_mask:0xf\n"
    "v_and_b32_dpp v23, v42, v3 row_newbcast:1 row_mask:0xf bank_mask:0xf\n"
    "v_and_b32_dpp v24, v42, v2 row_newbcast:2 row_mask:0xf bank_mask:0xf\n"
    "v_and_b32_dpp v25, v42, v3 row_newbcast:2 row_mask:0xf bank_mask:0xf\n"
    "v_and_b32_dpp v26, v42, v2 row_newbcast:3 row_mask:0xf bank_mask:0xf\n"
    "v_and_b32_dpp v27, v42, v3 row_newbcast:3 row_mask:0xf bank_mask:0xf\n"
    "v_smfmac_f32_16x16x64_bf16 v[36:39], v[20:23], v[56:63], v4\n"
    "v_and_b32_dpp v20, v42, v2 row_newbcast:4 row_mask:0xf bank_mask:0xf\n"
    "v_and_b32_dpp v21, v42, v3 row_newbcast:4 row_mask:0xf bank_mask:0xf\n"
    "v_and_b32_dpp v22, v42, v2 row_newbcast:5 row_mask:0xf bank_mask:0xf\n"
    "v_and_b32_dpp v23, v42, v3 row_newbcast:5 row_mask:0xf bank_mask:0xf\n"
    "v_smfmac_f32_16x16x64_bf16 v[36:39], v[24:27], v[64:71], v4\n"
    "v_and_b32_dpp v24, v42, v2 row_newbcast:6 row_mask:0xf bank_mask:0xf\n"
    "v_and_b32_dpp v25, v42, v3 row_newbcast:6 row_mask:0xf bank_mask:0xf\n"
    "v_and_b32_dpp v26, v42, v2 row_newbcast:7 row_mask:0xf bank_mask:0xf\n"
    "v_and_b32_dpp v27, v42, v3 row_newbcast:7 row_mask:0xf bank_mask:0xf\n"
    "v_smfmac_f32_16x16x64_bf16 v[36:39], v[20:23], v[72:79], v4\n"
    "v_and_b32_dpp v20, v42, v2 row_newbcast:8 row_mask:0xf bank_mask:0xf\n"
    "v_and_b32_dpp v21, v42, v3 row_newbcast:8 row_mask:0xf bank_mask:0xf\n"
    "v_and_b32_dpp v22, v42, v2 row_newbcast:9 row_mask:0xf bank_mask:0xf\n"
    "v_and_b32_dpp v23, v42, v3 row_newbcast:9 row_mask:0xf bank_mask:0xf\n"
    "v_smfmac_f32_16x16x64_bf16 v[36:39], v[24:27], v[80:87], v4\n"
    "v_and_b32_dpp v24, v42, v2 row_newbcast:10 row_mask:0xf bank_mask:0xf\n"
    "v_and_b32_dpp v25, v42, v3 row_newbcast:10 row_mask:0xf bank_mask:0xf\n"
    "v_and_b32_dpp v26, v42, v2 row_newbcast:11 row_mask:0xf bank_mask:0xf\n"
    "v_and_b32_dpp v27, v42, v3 row_newbcast:11 row_mask:0xf bank_mask:0xf\n"
    "v_smfmac_f32_16x16x64_bf16 v[36:39], v[20:23], v[88:95], v4\n"
    "v_and_b32_dpp v20, v42, v2 row_newbcast:12 row_mask:0xf bank_mask:0xf\n"
    "v_and_b32_dpp v21, v42, v3 row_newbcast:12 row_mask:0xf bank_mask:0xf\n"
    "v_and_b32_dpp v22, v42, v2 row_newbcast:13 row_mask:0xf bank_mask:0xf\n"
    "v_and_b32_dpp v23, v42, v3 row_newbcast:13 row_mask:0xf bank_mask:0xf\n"
    "v_smfmac_f32_16x16x64_bf16 v[36:39], v[24:27], v[96:103], v4\n"
    "v_and_b32_dpp v24, v42, v2 row_newbcast:14 row_mask:0xf bank_mask:0xf\n"
    "v_and_b32_dpp v25, v42, v3 row_newbcast:14 row_mask:0xf bank_mask:0xf\n"
    "v_and_b32_dpp v26, v42, v2 row_newbcast:15 row_mask:0xf bank_mask:0xf\n"
    "v_and_b32_dpp v27, v42, v3 row_newbcast:15 row_mask:0xf bank_mask:0xf\n"
    "v_smfmac_f32_16x16x64_bf16 v[36:39], v[20:23], v[104:111], v4\n"
    "s_nop 0\n"
    "v_smfmac_f32_16x16x64_bf16 v[36:39], v[24:27], v[112:119], v4\n"
    "v_mov_b32 v45, s68\n"
    "v_and_b32 v40, 0xff, v19\n"
    "v_lshrrev_b32 v41, 8, v19\n"
    "v_lshl_add_u32 v41, v41, 14, v40\n"
    "s_cmp_ge_u32 s64, 4\n"
    "s_nop 2\n"
    "ds_write_b128 v15, v[36:39] offset:0\n"
    "s_waitcnt lgkmcnt(0)\n"
    "s_barrier\n"
    "ds_read2_b64 v[20:23], v40 offset0:0 offset1:32\n"
    "ds_read2_b64 v[24:27], v40 offset0:64 offset1:96\n"
    "ds_read2_b64 v[28:31], v40 offset0:128 offset1:160\n"
    "ds_read2_b64 v[32:35], v40 offset0:192 offset1:224\n"
    "s_waitcnt lgkmcnt(3)\n"
    "v_pk_add_f32 v[20:21], v[20:21], v[22:23]\n"
    "v_pk_add_f32 v[46:47], v[46:47], v[20:21]\n"
    "s_waitcnt lgkmcnt(2)\n"
    "v_pk_add_f32 v[24:25], v[24:25], v[26:27]\n"
    "v_pk_add_f32 v[46:47], v[46:47], v[24:25]\n"
    "s_waitcnt lgkmcnt(1)\n"
    "v_pk_add_f32 v[28:29], v[28:29], v[30:31]\n"
    "v_pk_add_f32 v[46:47], v[46:47], v[28:29]\n"
    "s_waitcnt lgkmcnt(0)\n"
    "v_pk_add_f32 v[32:33], v[32:33], v[34:35]\n"
    "v_pk_add_f32 v[46:47], v[46:47], v[32:33]\n"
    "v_cvt_pk_bf16_f32 v44, v46, v47\n"
    "buffer_store_dwordx2 v[44:45], v41, s[32:35], s88 offen sc1\n"
    ".Lk_b_pub:\n"
    "global_load_dwordx4 v[56:59], v[10:11], off offset:-3584\n"
    "global_load_dwordx4 v[60:63], v[10:11], off offset:3584\n"
    "v_lshl_add_u64 v[10:11], v[10:11], 0, s[60:61]\n"
    "global_load_dwordx4 v[64:67], v[10:11], off offset:-3584\n"
    "global_load_dwordx4 v[68:71], v[10:11], off offset:3584\n"
    "v_lshl_add_u64 v[10:11], v[10:11], 0, s[60:61]\n"
    "global_load_dwordx4 v[72:75], v[10:11], off offset:-3584\n"
    "global_load_dwordx4 v[76:79], v[10:11], off offset:3584\n"
    "v_lshl_add_u64 v[10:11], v[10:11], 0, s[48:49]\n"
    "s_sleep 4\n"
    "buffer_load_dwordx2 v[20:21], v14, s[32:35], s89 offen offset:0 sc1\n"
    "buffer_load_dwordx2 v[22:23], v14, s[32:35], s89 offen offset:2048 sc1\n"
    "s_add_u32 s74, s89, 0x1000\n"
    "buffer_load_dwordx2 v[24:25], v14, s[32:35], s74 offen offset:0 sc1\n"
    "buffer_load_dwordx2 v[26:27], v14, s[32:35], s74 offen offset:2048 sc1\n"
    "s_add_u32 s73, s89, 0x2000\n"
    "buffer_load_dwordx2 v[28:29], v14, s[32:35], s73 offen offset:0 sc1\n"
    "buffer_load_dwordx2 v[30:31], v14, s[32:35], s73 offen offset:2048 sc1\n"
    "s_add_u32 s74, s89, 0x3000\n"
    "buffer_load_dwordx2 v[32:33], v14, s[32:35], s74 offen offset:0 sc1\n"
    "buffer_load_dwordx2 v[34:35], v14, s[32:35], s74 offen offset:2048 sc1\n"
    "buffer_load_dwordx4 v[80:83], v12, s[44:47], 0 offen offset:0\n"
    "s_mov_b32 s71, 0x3000\n"
    "buffer_load_dwordx4 v[84:87], v12, s[44:47], s71 offen offset:2048\n"
    "buffer_load_dwordx4 v[88:91], v12, s[44:47], 0 offen offset:1792\n"
    "s_mov_b32 s71, 0x3000\n"
    "buffer_load_dwordx4 v[92:95], v12, s[44:47], s71 offen offset:3840\n"
    "buffer_load_dwordx4 v[96:99], v12, s[44:47], 0 offen offset:3584\n"
    "s_mov_b32 s71, 0x4000\n"
    "buffer_load_dwordx4 v[100:103], v12, s[44:47], s71 offen offset:1536\n"
    "s_mov_b32 s72, 0x1000\n"
    "buffer_load_dwordx4 v[104:107], v12, s[44:47], s72 offen offset:1280\n"
    "s_mov_b32 s71, 0x4000\n"
    "buffer_load_dwordx4 v[108:111], v12, s[44:47], s71 offen offset:3328\n"
    "buffer_load_dwordx4 v[112:115], v8, s[36:39], 0 offen offset:0\n"
    "s_mov_b32 s71, 0x40000\n"
    "buffer_load_dwordx4 v[116:119], v8, s[36:39], s71 offen offset:0\n"
    "s_mul_i32 s71, s64, 224\n"
    "s_add_u32 s71, s71, 0x3c00\n"
    "v_lshrrev_b32 v40, 1, v14\n"
    "v_add_u32 v40, s71, v40\n"
    "v_lshrrev_b32 v41, 8, v1\n"
    "v_lshlrev_b32 v41, 5, v41\n"
    "v_and_b32 v42, 0x80, v1\n"
    "v_add_u32 v41, v41, v42\n"
    "v_bfe_u32 v42, v1, 4, 3\n"
    "v_lshl_add_u32 v41, v42, 2, v41\n"
    "s_lshl_b32 s72, s64, 3\n"
    "s_add_u32 s72, s72, 0x3fc0\n"
    "v_lshrrev_b32 v42, 7, v1\n"
    "v_add_u32 v42, s72, v42\n"
    "v_mov_b64 v[44:45], 0\n"
    "v_mov_b64 v[46:47], 0\n"
    "s_cmp_lg_u32 s69, 0\n"
    "s_cbranch_scc1 .Lk_b_use\n"
    "v_and_b32 v36, 0x3f803f80, v2\n"
    "v_and_b32 v37, 0x3f803f80, v3\n"
    "v_and_b32 v38, 0x3f803f80, v2\n"
    "v_and_b32 v39, 0x3f803f80, v3\n"
    "s_nop 1\n"
    "v_smfmac_f32_16x16x64_bf16 v[44:47], v[36:39], v[48:55], v4\n"
    "v_smfmac_f32_16x16x64_bf16 v[44:47], v[36:39], v[120:127], v4\n"
    "v_smfmac_f32_16x16x64_bf16 v[44:47], v[36:39], v[128:135], v4\n"
    "v_smfmac_f32_16x16x64_bf16 v[44:47], v[36:39], v[136:143], v4\n"
    "s_waitcnt vmcnt(22)\n"
    "v_smfmac_f32_16x16x64_bf16 v[44:47], v[36:39], v[56:63], v4\n"
    "s_waitcnt vmcnt(20)\n"
    "v_smfmac_f32_16x16x64_bf16 v[44:47], v[36:39], v[64:71], v4\n"
    "s_waitcnt vmcnt(18)\n"
    "v_smfmac_f32_16x16x64_bf16 v[44:47], v[36:39], v[72:79], v4\n"
    "s_nop 7\n"
    "ds_write_b128 v15, v[44:47] offset:8192\n"
    "s_waitcnt lgkmcnt(0)\n"
    "s_barrier\n"
    "ds_read2st64_b32 v[36:37], v41 offset0:32 offset1:33\n"
    "ds_read2st64_b32 v[38:39], v41 offset0:34 offset1:35\n"
    "ds_read2st64_b32 v[44:45], v41 offset0:36 offset1:37\n"
    "ds_read2st64_b32 v[46:47], v41 offset0:38 offset1:39\n"
    "s_waitcnt lgkmcnt(0)\n"
    "v_pk_add_f32 v[36:37], v[36:37], v[38:39]\n"
    "v_pk_add_f32 v[44:45], v[44:45], v[46:47]\n"
    "v_pk_add_f32 v[36:37], v[36:37], v[44:45]\n"
    "v_add_f32 v6, v36, v37\n"
    "v_mov_b64 v[44:45], 0\n"
    "v_mov_b64 v[46:47], 0\n"
    ".Lk_b_use:\n"
    "v_mov_b64 v[38:39], 0\n"
    "s_waitcnt vmcnt(17)\n"
    "v_dot2c_f32_bf16 v38, s62, v20\n"
    "v_dot2c_f32_bf16 v39, s63, v20\n"
    "v_xor_b32 v36, s68, v21\n"
    "s_waitcnt vmcnt(16)\n"
    "v_dot2c_f32_bf16 v38, s62, v22\n"
    "v_dot2c_f32_bf16 v39, s63, v22\n"
    "v_bitop3_b32 v36, v23, v36, s68 bitop3:0xde\n"
    "s_waitcnt vmcnt(15)\n"
    "v_dot2c_f32_bf16 v38, s62, v24\n"
    "v_dot2c_f32_bf16 v39, s63, v24\n"
    "v_bitop3_b32 v36, v25, v36, s68 bitop3:0xde\n"
    "s_waitcnt vmcnt(14)\n"
    "v_dot2c_f32_bf16 v38, s62, v26\n"
    "v_dot2c_f32_bf16 v39, s63, v26\n"
    "v_bitop3_b32 v36, v27, v36, s68 bitop3:0xde\n"
    "s_waitcnt vmcnt(13)\n"
    "v_dot2c_f32_bf16 v38, s62, v28\n"
    "v_dot2c_f32_bf16 v39, s63, v28\n"
    "v_bitop3_b32 v36, v29, v36, s68 bitop3:0xde\n"
    "s_waitcnt vmcnt(12)\n"
    "v_dot2c_f32_bf16 v38, s62, v30\n"
    "v_dot2c_f32_bf16 v39, s63, v30\n"
    "v_bitop3_b32 v36, v31, v36, s68 bitop3:0xde\n"
    "s_waitcnt vmcnt(11)\n"
    "v_dot2c_f32_bf16 v38, s62, v32\n"
    "v_dot2c_f32_bf16 v39, s63, v32\n"
    "v_bitop3_b32 v36, v33, v36, s68 bitop3:0xde\n"
    "s_waitcnt vmcnt(10)\n"
    "v_dot2c_f32_bf16 v38, s62, v34\n"
    "v_dot2c_f32_bf16 v39, s63, v34\n"
    "v_bitop3_b32 v36, v35, v36, s68 bitop3:0xde\n"
    "v_cmp_ne_u32 vcc, 0, v36\n"
    "s_cbranch_vccnz .Lk_b_retry\n"
    ".Lk_b_ok:\n"
    "v_cvt_pk_bf16_f32 v37, v38, v39\n"
    "v_mov_b32 v30, 0x3f803f80\n"
    "v_cndmask_b32 v31, 0, v37, s[98:99]\n"
    "v_and_b32_dpp v20, v37, v2 row_newbcast:0 row_mask:0xf bank_mask:0xf\n"
    "v_and_b32_dpp v21, v37, v3 row_newbcast:0 row_mask:0xf bank_mask:0xf\n"
    "v_and_b32_dpp v22, v37, v2 row_newbcast:1 row_mask:0xf bank_mask:0xf\n"
    "v_and_b32_dpp v23, v37, v3 row_newbcast:1 row_mask:0xf bank_mask:0xf\n"
    "v_and_b32_dpp v24, v37, v2 row_newbcast:2 row_mask:0xf bank_mask:0xf\n"
    "v_and_b32_dpp v25, v37, v3 row_newbcast:2 row_mask:0xf bank_mask:0xf\n"
    "v_and_b32_dpp v26, v37, v2 row_newbcast:3 row_mask:0xf bank_mask:0xf\n"
    "v_and_b32_dpp v27, v37, v3 row_newbcast:3 row_mask:0xf bank_mask:0xf\n"
    "ds_write_b32 v40, v37\n"
    "v_dot2_f32_bf16 v28, v31, v30, 0\n"
    "v_smfmac_f32_16x16x64_bf16 v[44:47], v[20:23], v[48:55], v4\n"
    "v_and_b32_dpp v20, v37, v2 row_newbcast:4 row_mask:0xf bank_mask:0xf\n"
    "v_and_b32_dpp v21, v37, v3 row_newbcast:4 row_mask:0xf bank_mask:0xf\n"
    "v_and_b32_dpp v22, v37, v2 row_newbcast:5 row_mask:0xf bank_mask:0xf\n"
    "v_and_b32_dpp v23, v37, v3 row_newbcast:5 row_mask:0xf bank_mask:0xf\n"
    "v_dot2_f32_bf16 v29, v31, v31, 0\n"
    "s_nop 2\n"
    "v_permlane32_swap_b32 v28, v29\n"
    "v_smfmac_f32_16x16x64_bf16 v[44:47], v[24:27], v[120:127], v4\n"
    "v_and_b32_dpp v24, v37, v2 row_newbcast:6 row_mask:0xf bank_mask:0xf\n"
    "v_and_b32_dpp v25, v37, v3 row_newbcast:6 row_mask:0xf bank_mask:0xf\n"
    "v_and_b32_dpp v26, v37, v2 row_newbcast:7 row_mask:0xf bank_mask:0xf\n"
    "v_and_b32_dpp v27, v37, v3 row_newbcast:7 row_mask:0xf bank_mask:0xf\n"
    "v_add_f32 v28, v28, v29\n"
    "v_mov_b32 v30, v28\n"
    "v_smfmac_f32_16x16x64_bf16 v[44:47], v[20:23], v[128:135], v4\n"
    "v_and_b32_dpp v20, v37, v2 row_newbcast:8 row_mask:0xf bank_mask:0xf\n"
    "v_and_b32_dpp v21, v37, v3 row_newbcast:8 row_mask:0xf bank_mask:0xf\n"
    "v_and_b32_dpp v22, v37, v2 row_newbcast:9 row_mask:0xf bank_mask:0xf\n"
    "v_and_b32_dpp v23, v37, v3 row_newbcast:9 row_mask:0xf bank_mask:0xf\n"
    "v_permlane16_swap_b32 v28, v30\n"
    "v_smfmac_f32_16x16x64_bf16 v[44:47], v[24:27], v[136:143], v4\n"
    "v_and_b32_dpp v24, v37, v2 row_newbcast:10 row_mask:0xf bank_mask:0xf\n"
    "v_and_b32_dpp v25, v37, v3 row_newbcast:10 row_mask:0xf bank_mask:0xf\n"
    "v_and_b32_dpp v26, v37, v2 row_newbcast:11 row_mask:0xf bank_mask:0xf\n"
    "v_and_b32_dpp v27, v37, v3 row_newbcast:11 row_mask:0xf bank_mask:0xf\n"
    "v_add_f32 v28, v28, v30\n"
    "v_smfmac_f32_16x16x64_bf16 v[44:47], v[20:23], v[56:63], v4\n"
    "v_and_b32_dpp v20, v37, v2 row_newbcast:12 row_mask:0xf bank_mask:0xf\n"
    "v_and_b32_dpp v21, v37, v3 row_newbcast:12 row_mask:0xf bank_mask:0xf\n"
    "v_and_b32_dpp v22, v37, v2 row_newbcast:13 row_mask:0xf bank_mask:0xf\n"
    "v_and_b32_dpp v23, v37, v3 row_newbcast:13 row_mask:0xf bank_mask:0xf\n"
    "v_add_f32_dpp v28, v28, v28 row_ror:8 row_mask:0xf bank_mask:0xf\n"
    "v_smfmac_f32_16x16x64_bf16 v[44:47], v[24:27], v[64:71], v4\n"
    "s_nop 0\n"
    "v_add_f32_dpp v28, v28, v28 row_ror:4 row_mask:0xf bank_mask:0xf\n"
    "v_smfmac_f32_16x16x64_bf16 v[44:47], v[20:23], v[72:79], v4\n"
    "s_nop 0\n"
    "v_add_f32_dpp v28, v28, v28 row_ror:2 row_mask:0xf bank_mask:0xf\n"
    "v_mov_b32 v32, 0\n"
    "v_mov_b32 v33, 0x3727c5ac\n"
    "v_add_f32_dpp v28, v28, v28 row_ror:1 row_mask:0xf bank_mask:0xf\n"
    "s_mov_b64 exec, s[96:97]\n"
    "ds_write_b32 v42, v28\n"
    "s_mov_b64 exec, -1\n"
    "ds_write_b128 v15, v[44:47] offset:4096\n"
    "v_mov_b32 v42, 0x3fc0\n"
    "v_mov_b64 v[36:37], 0\n"
    "v_mov_b64 v[38:39], 0\n"
    "s_mov_b32 s94, 0x3b124925\n"
    "s_mov_b32 s95, 0x3b124925\n"
    "s_cmp_lg_u32 s69, 0\n"
    "s_cbranch_scc1 .Lk_nowq0\n"
    "s_mov_b32 s72, 0x4000\n"
    "buffer_load_dwordx4 v[48:51], v8, s[36:39], s72 offen offset:0\n"
    "s_mov_b32 s71, 0x44000\n"
    "buffer_load_dwordx4 v[52:55], v8, s[36:39], s71 offen offset:0\n"
    ".Lk_nowq0:\n"
    "s_waitcnt lgkmcnt(0)\n"
    "s_barrier\n"
    "ds_read_b128 v[20:23], v42\n"
    "ds_read_b128 v[24:27], v42 offset:16\n"
    "ds_read2st64_b32 v[28:29], v41 offset0:16 offset1:17\n"
    "ds_read2st64_b32 v[30:31], v41 offset0:18 offset1:19\n"
    "ds_read2st64_b32 v[34:35], v41 offset0:20 offset1:21\n"
    "ds_read2st64_b32 v[46:47], v41 offset0:22 offset1:23\n"
    "s_cbranch_scc1 .Lk_nowq1\n"
    "s_mov_b32 s72, 0x8000\n"
    "buffer_load_dwordx4 v[120:123], v8, s[36:39], s72 offen offset:0\n"
    "s_mov_b32 s71, 0x48000\n"
    "buffer_load_dwordx4 v[124:127], v8, s[36:39], s71 offen offset:0\n"
    ".Lk_nowq1:\n"
    "v_mov_b32 v43, 0xbdd2d3e8\n"
    "v_lshrrev_b32 v40, 8, v13\n"
    "v_mul_u32_u24 v40, 0x3700, v40\n"
    "v_add_u32 v40, v40, v13\n"
    "s_waitcnt lgkmcnt(4)\n"
    "v_pk_add_f32 v[20:21], v[20:21], v[22:23]\n"
    "v_pk_add_f32 v[24:25], v[24:25], v[26:27]\n"
    "v_pk_add_f32 v[20:21], v[20:21], v[24:25]\n"
    "v_pk_fma_f32 v[20:21], v[20:21], s[94:95], v[32:33]\n"
    "v_fma_f32 v21, -v20, v20, v21\n"
    "v_max_f32 v21, v33, v21\n"
    "v_rsq_f32 v21, v21\n"
    "s_waitcnt lgkmcnt(0)\n"
    "v_pk_add_f32 v[28:29], v[28:29], v[30:31]\n"
    "v_pk_add_f32 v[34:35], v[34:35], v[46:47]\n"
    "v_pk_add_f32 v[28:29], v[28:29], v[34:35]\n"
    "v_fma_f32 v28, -v20, v6, v28\n"
    "v_add_f32 v28, v28, v29\n"
    "v_mul_f32 v28, v28, v21\n"
    "v_mul_f32 v29, v28, v28\n"
    "v_fmaak_f32 v29, v29, v43, 0xc0135761\n"
    "v_mul_f32 v29, v29, v28\n"
    "v_exp_f32 v29, v29\n"
    "v_mul_f32_dpp v30, v28, v28 row_ror:8 row_mask:0xf bank_mask:0xf\n"
    "v_add_f32 v29, 1.0, v29\n"
    "v_rcp_f32 v29, v29\n"
    "v_mov_b32 v45, s68\n"
    "v_mul_f32 v30, v30, v29\n"
    "v_cvt_pk_bf16_f32 v30, v30, v30\n"
    "v_mov_b32 v47, s68\n"
    "s_waitcnt vmcnt(6)\n"
    "s_cbranch_scc0 .Lk_wd1\n"
    "s_waitcnt vmcnt(2)\n"
    ".Lk_wd1:\n"
    "v_and_b32_dpp v20, v30, v2 row_newbcast:0 row_mask:0xf bank_mask:0xf\n"
    "v_and_b32_dpp v21, v30, v3 row_newbcast:0 row_mask:0xf bank_mask:0xf\n"
    "v_and_b32_dpp v22, v30, v2 row_newbcast:1 row_mask:0xf bank_mask:0xf\n"
    "v_and_b32_dpp v23, v30, v3 row_newbcast:1 row_mask:0xf bank_mask:0xf\n"
    "v_and_b32_dpp v24, v30, v2 row_newbcast:2 row_mask:0xf bank_mask:0xf\n"
    "v_and_b32_dpp v25, v30, v3 row_newbcast:2 row_mask:0xf bank_mask:0xf\n"
    "v_and_b32_dpp v26, v30, v2 row_newbcast:3 row_mask:0xf bank_mask:0xf\n"
    "v_and_b32_dpp v27, v30, v3 row_newbcast:3 row_mask:0xf bank_mask:0xf\n"
    "v_smfmac_f32_16x16x64_bf16 v[36:39], v[20:23], v[80:87], v4\n"
    "v_and_b32_dpp v20, v30, v2 row_newbcast:4 row_mask:0xf bank_mask:0xf\n"
    "v_and_b32_dpp v21, v30, v3 row_newbcast:4 row_mask:0xf bank_mask:0xf\n"
    "v_and_b32_dpp v22, v30, v2 row_newbcast:5 row_mask:0xf bank_mask:0xf\n"
    "v_and_b32_dpp v23, v30, v3 row_newbcast:5 row_mask:0xf bank_mask:0xf\n"
    "v_smfmac_f32_16x16x64_bf16 v[36:39], v[24:27], v[88:95], v4\n"
    "v_and_b32_dpp v24, v30, v2 row_newbcast:6 row_mask:0xf bank_mask:0xf\n"
    "v_and_b32_dpp v25, v30, v3 row_newbcast:6 row_mask:0xf bank_mask:0xf\n"
    "v_and_b32_dpp v26, v30, v2 row_newbcast:7 row_mask:0xf bank_mask:0xf\n"
    "v_and_b32_dpp v27, v30, v3 row_newbcast:7 row_mask:0xf bank_mask:0xf\n"
    "v_smfmac_f32_16x16x64_bf16 v[36:39], v[20:23], v[96:103], v4\n"
    "s_nop 0\n"
    "v_smfmac_f32_16x16x64_bf16 v[36:39], v[24:27], v[104:111], v4\n"
    "s_mov_b32 exec_hi, 0\n"
    "s_nop 6\n"
    "v_cvt_pk_bf16_f32 v44, v36, v37\n"
    "v_cvt_pk_bf16_f32 v46, v38, v39\n"
    "buffer_store_dwordx4 v[44:47], v40, s[32:35], s90 offen sc1\n"
    "s_mov_b32 exec_hi, -1\n"
    "s_cmp_lg_u32 s69, 0\n"
    "s_cbranch_scc0 .Lk_r_l1\n"
    "s_cmp_lg_u32 s65, s66\n"
    "s_cbranch_scc1 .Lk_end\n"
    "s_mov_b32 s42, 0\n"
    "s_sleep 12\n"
    "s_branch .Lk_r_go\n"
    ".Lk_r_l1:\n"
    "s_mov_b32 s72, 0xc000\n"
    "buffer_load_dwordx4 v[128:131], v8, s[36:39], s72 offen offset:0\n"
    "s_mov_b32 s71, 0x4c000\n"
    "buffer_load_dwordx4 v[132:135], v8, s[36:39], s71 offen offset:0\n"
    "s_mov_b32 s72, 0x10000\n"
    "buffer_load_dwordx4 v[136:139], v8, s[36:39], s72 offen offset:0\n"
    "s_mov_b32 s71, 0x50000\n"
    "buffer_load_dwordx4 v[140:143], v8, s[36:39], s71 offen offset:0\n"
    "s_mov_b32 s72, 0x14000\n"
    "buffer_load_dwordx4 v[56:59], v8, s[36:39], s72 offen offset:0\n"
    "s_mov_b32 s71, 0x54000\n"
    "buffer_load_dwordx4 v[60:63], v8, s[36:39], s71 offen offset:0\n"
    "s_mov_b32 s72, 0x18000\n"
    "buffer_load_dwordx4 v[64:67], v8, s[36:39], s72 offen offset:0\n"
    "s_mov_b32 s71, 0x58000\n"
    "buffer_load_dwordx4 v[68:71], v8, s[36:39], s71 offen offset:0\n"
    "s_mov_b32 s72, 0x1c000\n"
    "buffer_load_dwordx4 v[72:75], v8, s[36:39], s72 offen offset:0\n"
    "s_mov_b32 s71, 0x5c000\n"
    "buffer_load_dwordx4 v[76:79], v8, s[36:39], s71 offen offset:0\n"
    "s_sleep 4\n"
    ".Lk_r_go:\n"
    "buffer_load_dwordx2 v[20:21], v19, s[32:35], s91 offen offset:0 sc1\n"
    "buffer_load_dwordx2 v[22:23], v19, s[32:35], s91 offen offset:512 sc1\n"
    "buffer_load_dwordx2 v[24:25], v19, s[32:35], s91 offen offset:1024 sc1\n"
    "buffer_load_dwordx2 v[26:27], v19, s[32:35], s91 offen offset:1536 sc1\n"
    "buffer_load_dwordx2 v[28:29], v19, s[32:35], s91 offen offset:2048 sc1\n"
    "buffer_load_dwordx2 v[30:31], v19, s[32:35], s91 offen offset:2560 sc1\n"
    "buffer_load_dwordx2 v[32:33], v19, s[32:35], s91 offen offset:3072 sc1\n"
    "s_mov_b32 s72, 0x2000\n"
    "buffer_load_dwordx4 v[80:83], v9, s[40:43], s72 offen offset:2560\n"
    "s_mov_b32 s71, 0x10000\n"
    "buffer_load_dwordx4 v[84:87], v9, s[40:43], s71 offen offset:2560\n"
    "s_mov_b32 s72, 0x3000\n"
    "buffer_load_dwordx4 v[88:91], v9, s[40:43], s72 offen offset:2048\n"
    "s_mov_b32 s71, 0x11000\n"
    "buffer_load_dwordx4 v[92:95], v9, s[40:43], s71 offen offset:2048\n"
    "s_mov_b32 s72, 0x4000\n"
    "buffer_load_dwordx4 v[96:99], v9, s[40:43], s72 offen offset:1536\n"
    "s_mov_b32 s71, 0x12000\n"
    "buffer_load_dwordx4 v[100:103], v9, s[40:43], s71 offen offset:1536\n"
    "s_mov_b32 s72, 0x5000\n"
    "buffer_load_dwordx4 v[104:107], v9, s[40:43], s72 offen offset:1024\n"
    "s_mov_b32 s71, 0x13000\n"
    "buffer_load_dwordx4 v[108:111], v9, s[40:43], s71 offen offset:1024\n"
    "s_lshl_b32 s73, s66, 7\n"
    "s_add_u32 s73, s73, 0x3c00\n"
    "v_lshrrev_b32 v38, 2, v19\n"
    "v_add_u32 v38, s73, v38\n"
    "v_mov_b32 v35, 0\n"
    "ds_read_u16_d16_hi v35, v38\n"
    "s_lshl_b32 s73, s64, 9\n"
    "s_add_u32 s73, s73, 0x3000\n"
    "v_add_u32 v39, s73, v19\n"
    "v_mov_b64 v[40:41], 0\n"
    "v_mov_b64 v[42:43], 0\n"
    "v_lshrrev_b32 v38, 1, v19\n"
    ".Lk_r_use:\n"
    "v_mov_b64 v[36:37], 0\n"
    "s_waitcnt vmcnt(14)\n"
    "v_dot2c_f32_bf16 v36, s62, v20\n"
    "v_dot2c_f32_bf16 v37, s63, v20\n"
    "v_xor_b32 v34, s68, v21\n"
    "s_waitcnt vmcnt(13)\n"
    "v_dot2c_f32_bf16 v36, s62, v22\n"
    "v_dot2c_f32_bf16 v37, s63, v22\n"
    "v_bitop3_b32 v34, v23, v34, s68 bitop3:0xde\n"
    "s_waitcnt vmcnt(12)\n"
    "v_dot2c_f32_bf16 v36, s62, v24\n"
    "v_dot2c_f32_bf16 v37, s63, v24\n"
    "v_bitop3_b32 v34, v25, v34, s68 bitop3:0xde\n"
    "s_waitcnt vmcnt(11)\n"
    "v_dot2c_f32_bf16 v36, s62, v26\n"
    "v_dot2c_f32_bf16 v37, s63, v26\n"
    "v_bitop3_b32 v34, v27, v34, s68 bitop3:0xde\n"
    "s_waitcnt vmcnt(10)\n"
    "v_dot2c_f32_bf16 v36, s62, v28\n"
    "v_dot2c_f32_bf16 v37, s63, v28\n"
    "v_bitop3_b32 v34, v29, v34, s68 bitop3:0xde\n"
    "s_waitcnt vmcnt(9)\n"
    "v_dot2c_f32_bf16 v36, s62, v30\n"
    "v_dot2c_f32_bf16 v37, s63, v30\n"
    "v_bitop3_b32 v34, v31, v34, s68 bitop3:0xde\n"
    "s_waitcnt vmcnt(8)\n"
    "v_dot2c_f32_bf16 v36, s62, v32\n"
    "v_dot2c_f32_bf16 v37, s63, v32\n"
    "v_bitop3_b32 v34, v33, v34, s68 bitop3:0xde\n"
    "v_cmp_ne_u32 vcc, 0, v34\n"
    "s_cbranch_vccnz .Lk_r_retry\n"
    ".Lk_r_ok:\n"
    "ds_write_b64 v39, v[36:37]\n"
    "s_cmp_lg_u32 s69, 0\n"
    "s_waitcnt lgkmcnt(0)\n"
    "s_barrier\n"
    "ds_read2st64_b32 v[20:21], v38 offset0:48 offset1:49\n"
    "ds_read2st64_b32 v[22:23], v38 offset0:50 offset1:51\n"
    "ds_read2st64_b32 v[24:25], v38 offset0:52 offset1:53\n"
    "ds_read2st64_b32 v[26:27], v38 offset0:54 offset1:55\n"
    "s_waitcnt lgkmcnt(2)\n"
    "v_pk_add_f32 v[20:21], v[20:21], v[22:23]\n"
    "v_add_f32 v35, v35, v20\n"
    "v_add_f32 v35, v35, v21\n"
    "s_waitcnt lgkmcnt(0)\n"
    "v_pk_add_f32 v[24:25], v[24:25], v[26:27]\n"
    "v_add_f32 v35, v35, v24\n"
    "v_add_f32 v35, v35, v25\n"
    "v_cvt_pk_bf16_f32 v28, v35, v35\n"
    "s_cbranch_scc1 .Lk_final\n"
    "v_and_b32 v32, 0xffff0000, v28\n"
    "v_and_b32_dpp v20, v28, v2 row_newbcast:0 row_mask:0xf bank_mask:0xf\n"
    "v_and_b32_dpp v21, v28, v3 row_newbcast:0 row_mask:0xf bank_mask:0xf\n"
    "v_and_b32_dpp v22, v28, v2 row_newbcast:1 row_mask:0xf bank_mask:0xf\n"
    "v_and_b32_dpp v23, v28, v3 row_newbcast:1 row_mask:0xf bank_mask:0xf\n"
    "v_and_b32_dpp v24, v28, v2 row_newbcast:2 row_mask:0xf bank_mask:0xf\n"
    "v_and_b32_dpp v25, v28, v3 row_newbcast:2 row_mask:0xf bank_mask:0xf\n"
    "v_and_b32_dpp v26, v28, v2 row_newbcast:3 row_mask:0xf bank_mask:0xf\n"
    "v_and_b32_dpp v27, v28, v3 row_newbcast:3 row_mask:0xf bank_mask:0xf\n"
    "v_mul_f32 v29, v32, v32\n"
    "s_add_u32 s32, s32, 0x91000\n"
    "s_addc_u32 s33, s33, 0\n"
    "s_mov_b32 s34, 0x16f000\n"
    "v_smfmac_f32_16x16x64_bf16 v[40:43], v[20:23], v[112:119], v4\n"
    "v_and_b32_dpp v20, v28, v2 row_newbcast:4 row_mask:0xf bank_mask:0xf\n"
    "v_and_b32_dpp v21, v28, v3 row_newbcast:4 row_mask:0xf bank_mask:0xf\n"
    "v_and_b32_dpp v22, v28, v2 row_newbcast:5 row_mask:0xf bank_mask:0xf\n"
    "v_and_b32_dpp v23, v28, v3 row_newbcast:5 row_mask:0xf bank_mask:0xf\n"
    "v_permlane32_swap_b32 v32, v29\n"
    "v_add_f32 v32, v32, v29\n"
    "v_smfmac_f32_16x16x64_bf16 v[40:43], v[24:27], v[48:55], v4\n"
    "v_and_b32_dpp v24, v28, v2 row_newbcast:6 row_mask:0xf bank_mask:0xf\n"
    "v_and_b32_dpp v25, v28, v3 row_newbcast:6 row_mask:0xf bank_mask:0xf\n"
    "v_and_b32_dpp v26, v28, v2 row_newbcast:7 row_mask:0xf bank_mask:0xf\n"
    "v_and_b32_dpp v27, v28, v3 row_newbcast:7 row_mask:0xf bank_mask:0xf\n"
    "v_mov_b32 v30, v32\n"
    "s_nop 1\n"
    "v_permlane16_swap_b32 v32, v30\n"
    "v_smfmac_f32_16x16x64_bf16 v[40:43], v[20:23], v[120:127], v4\n"
    "v_and_b32_dpp v20, v28, v2 row_newbcast:8 row_mask:0xf bank_mask:0xf\n"
    "v_and_b32_dpp v21, v28, v3 row_newbcast:8 row_mask:0xf bank_mask:0xf\n"
    "v_and_b32_dpp v22, v28, v2 row_newbcast:9 row_mask:0xf bank_mask:0xf\n"
    "v_and_b32_dpp v23, v28, v3 row_newbcast:9 row_mask:0xf bank_mask:0xf\n"
    "v_add_f32 v32, v32, v30\n"
    "v_smfmac_f32_16x16x64_bf16 v[40:43], v[24:27], v[128:135], v4\n"
    "v_and_b32_dpp v24, v28, v2 row_newbcast:10 row_mask:0xf bank_mask:0xf\n"
    "v_and_b32_dpp v25, v28, v3 row_newbcast:10 row_mask:0xf bank_mask:0xf\n"
    "v_and_b32_dpp v26, v28, v2 row_newbcast:11 row_mask:0xf bank_mask:0xf\n"
    "v_and_b32_dpp v27, v28, v3 row_newbcast:11 row_mask:0xf bank_mask:0xf\n"
    "v_add_f32_dpp v32, v32, v32 row_ror:8 row_mask:0xf bank_mask:0xf\n"
    "v_smfmac_f32_16x16x64_bf16 v[40:43], v[20:23], v[136:143], v4\n"
    "v_and_b32_dpp v20, v28, v2 row_newbcast:12 row_mask:0xf bank_mask:0xf\n"
    "v_and_b32_dpp v21, v28, v3 row_newbcast:12 row_mask:0xf bank_mask:0xf\n"
    "v_and_b32_dpp v22, v28, v2 row_newbcast:13 row_mask:0xf bank_mask:0xf\n"
    "v_and_b32_dpp v23, v28, v3 row_newbcast:13 row_mask:0xf bank_mask:0xf\n"
    "v_add_f32_dpp v32, v32, v32 row_ror:4 row_mask:0xf bank_mask:0xf\n"
    "v_smfmac_f32_16x16x64_bf16 v[40:43], v[24:27], v[56:63], v4\n"
    "v_and_b32_dpp v24, v28, v2 row_newbcast:14 row_mask:0xf bank_mask:0xf\n"
    "v_and_b32_dpp v25, v28, v3 row_newbcast:14 row_mask:0xf bank_mask:0xf\n"
    "v_and_b32_dpp v26, v28, v2 row_newbcast:15 row_mask:0xf bank_mask:0xf\n"
    "v_and_b32_dpp v27, v28, v3 row_newbcast:15 row_mask:0xf bank_mask:0xf\n"
    "v_add_f32_dpp v32, v32, v32 row_ror:2 row_mask:0xf bank_mask:0xf\n"
    "v_smfmac_f32_16x16x64_bf16 v[40:43], v[20:23], v[64:71], v4\n"
    "s_nop 0\n"
    "v_add_f32_dpp v32, v32, v32 row_ror:1 row_mask:0xf bank_mask:0xf\n"
    "v_smfmac_f32_16x16x64_bf16 v[40:43], v[24:27], v[72:79], v4\n"
    "v_readlane_b32 s73, v32, 32\n"
    "v_mov_b32 v33, s68\n"
    "v_mov_b32 v35, s68\n"
    "s_mov_b64 exec, 0xff\n"
    "v_mov_b32 v34, s73\n"
    "buffer_store_dwordx4 v[32:35], v1, s[32:35], s81 offen sc1\n"
    "s_mov_b32 exec_lo, -1\n"
    "s_nop 0\n"
    "v_cvt_pk_bf16_f32 v44, v40, v41\n"
    "v_cvt_pk_bf16_f32 v46, v42, v43\n"
    "buffer_store_dwordx4 v[44:47], v13, s[32:35], s80 offen sc1\n"
    "s_mov_b32 exec_hi, -1\n"
    "buffer_load_dwordx4 v[56:59], v9, s[40:43], 0 offen offset:0\n"
    "s_mov_b32 s71, 0xe000\n"
    "buffer_load_dwordx4 v[60:63], v9, s[40:43], s71 offen offset:0\n"
    "buffer_load_dwordx4 v[64:67], v9, s[40:43], 0 offen offset:3584\n"
    "s_mov_b32 s71, 0xe000\n"
    "buffer_load_dwordx4 v[68:71], v9, s[40:43], s71 offen offset:3584\n"
    "s_mov_b32 s72, 0x1000\n"
    "buffer_load_dwordx4 v[72:75], v9, s[40:43], s72 offen offset:3072\n"
    "s_mov_b32 s71, 0xf000\n"
    "buffer_load_dwordx4 v[76:79], v9, s[40:43], s71 offen offset:3072\n"
    "s_mov_b32 s72, 0x6000\n"
    "buffer_load_dwordx4 v[112:115], v9, s[40:43], s72 offen offset:512\n"
    "s_mov_b32 s71, 0x14000\n"
    "buffer_load_dwordx4 v[116:119], v9, s[40:43], s71 offen offset:512\n"
    "v_lshrrev_b32 v20, 2, v19\n"
    "ds_write_b16 v20, v28 offset:2048\n"
    "s_cmp_eq_u32 s65, s66\n"
    "s_cselect_b32 s73, -1, 0\n"
    "v_and_b32 v38, 127, v38\n"
    "ds_read_b32 v7, v38 offset:2048\n"
    "s_waitcnt lgkmcnt(0)\n"
    "v_and_b32 v7, s73, v7\n"
    "s_sleep 13\n"
    ".Lk_q_go:\n"
    "s_mov_b32 s69, 16\n"
    "v_bfe_u32 v28, v1, 4, 3\n"
    "v_min_u32 v28, 6, v28\n"
    "v_lshlrev_b32 v28, 7, v28\n"
    "buffer_load_dwordx4 v[32:35], v28, s[32:35], s83 offen offset:0 sc1\n"
    "buffer_load_dwordx2 v[40:41], v19, s[32:35], s82 offen offset:0 sc1\n"
    "buffer_load_dwordx2 v[42:43], v19, s[32:35], s82 offen offset:512 sc1\n"
    "buffer_load_dwordx2 v[44:45], v19, s[32:35], s82 offen offset:1024 sc1\n"
    "buffer_load_dwordx2 v[46:47], v19, s[32:35], s82 offen offset:1536 sc1\n"
    "buffer_load_dwordx2 v[48:49], v19, s[32:35], s82 offen offset:2048 sc1\n"
    "buffer_load_dwordx2 v[50:51], v19, s[32:35], s82 offen offset:2560 sc1\n"
    "buffer_load_dwordx2 v[52:53], v19, s[32:35], s82 offen offset:3072 sc1\n"
    "v_add_u32 v36, s75, v1\n"
    "ds_read_b128 v[120:123], v36 offset:0\n"
    "ds_read_b128 v[124:127], v36 offset:1024\n"
    "ds_read_b128 v[128:131], v36 offset:2048\n"
    "ds_read_b128 v[132:135], v36 offset:3072\n"
    "ds_read_b128 v[136:139], v36 offset:4096\n"
    "ds_read_b128 v[140:143], v36 offset:5120\n"
    "v_mov_b64 v[20:21], 0\n"
    "v_mov_b64 v[22:23], 0\n"
    "v_mov_b64 v[24:25], 0\n"
    "v_mov_b64 v[26:27], 0\n"
    ".Lk_q_use:\n"
    "s_mov_b32 s94, 0x7f7f7f7f\n"
    "s_mov_b32 s95, 0x7f7f7f7f\n"
    "v_lshlrev_b32 v30, 16, v5\n"
    "v_and_b32 v31, 0xffff0000, v5\n"
    "s_waitcnt vmcnt(7)\n"
    "v_cndmask_b32 v54, 0, v32, s[94:95]\n"
    "v_cndmask_b32 v55, 0, v34, s[94:95]\n"
    "v_xor_b32 v29, s68, v33\n"
    "v_add_f32_dpp v54, v54, v54 row_half_mirror row_mask:0xf bank_mask:0xf\n"
    "v_add_f32_dpp v55, v55, v55 row_half_mirror row_mask:0xf bank_mask:0xf\n"
    "v_bitop3_b32 v29, v35, v29, s68 bitop3:0xde\n"
    "v_add_f32_dpp v54, v54, v54 quad_perm:[2,3,0,1] row_mask:0xf bank_mask:0xf\n"
    "v_add_f32_dpp v55, v55, v55 quad_perm:[2,3,0,1] row_mask:0xf bank_mask:0xf\n"
    "s_nop 0\n"
    "v_add_f32_dpp v54, v54, v54 quad_perm:[1,0,3,2] row_mask:0xf bank_mask:0xf\n"
    "v_add_f32_dpp v55, v55, v55 quad_perm:[1,0,3,2] row_mask:0xf bank_mask:0xf\n"
    "v_mul_f32 v54, 0xbb124925, v54\n"
    "v_pk_mul_f32 v[30:31], v[30:31], v[54:55] op_sel_hi:[1,0]\n"
    "v_mul_f32 v55, 0x3b124925, v55\n"
    "s_waitcnt vmcnt(6)\n"
    "v_dot2c_f32_bf16 v30, s62, v40\n"
    "v_dot2c_f32_bf16 v31, s63, v40\n"
    "v_bitop3_b32 v29, v41, v29, s68 bitop3:0xde\n"
    "v_fma_f32 v55, -v54, v54, v55\n"
    "s_waitcnt vmcnt(5)\n"
    "v_dot2c_f32_bf16 v30, s62, v42\n"
    "v_dot2c_f32_bf16 v31, s63, v42\n"
    "v_bitop3_b32 v29, v43, v29, s68 bitop3:0xde\n"
    "v_max_f32 v55, 0, v55\n"
    "s_waitcnt vmcnt(4)\n"
    "v_dot2c_f32_bf16 v30, s62, v44\n"
    "v_dot2c_f32_bf16 v31, s63, v44\n"
    "v_bitop3_b32 v29, v45, v29, s68 bitop3:0xde\n"
    "v_add_f32 v55, 0x3727c5ac, v55\n"
    "s_waitcnt vmcnt(3)\n"
    "v_dot2c_f32_bf16 v30, s62, v46\n"
    "v_dot2c_f32_bf16 v31, s63, v46\n"
    "v_bitop3_b32 v29, v47, v29, s68 bitop3:0xde\n"
    "v_rsq_f32 v55, v55\n"
    "s_waitcnt vmcnt(2)\n"
    "v_dot2c_f32_bf16 v30, s62, v48\n"
    "v_dot2c_f32_bf16 v31, s63, v48\n"
    "v_bitop3_b32 v29, v49, v29, s68 bitop3:0xde\n"
    "v_mul_f32 v55, 0x3db8aa3b, v55\n"
    "s_waitcnt vmcnt(1)\n"
    "v_dot2c_f32_bf16 v30, s62, v50\n"
    "v_dot2c_f32_bf16 v31, s63, v50\n"
    "v_bitop3_b32 v29, v51, v29, s68 bitop3:0xde\n"
    "s_waitcnt vmcnt(0)\n"
    "v_dot2c_f32_bf16 v30, s62, v52\n"
    "v_dot2c_f32_bf16 v31, s63, v52\n"
    "v_bitop3_b32 v29, v53, v29, s68 bitop3:0xde\n"
    "v_cmp_ne_u32 vcc, 0, v29\n"
    "s_cbranch_vccnz .Lk_q_retry\n"
    ".Lk_q_ok:\n"
    "v_pk_mul_f32 v[30:31], v[30:31], v[54:55] op_sel:[0,1] op_sel_hi:[1,1]\n"
    "v_cvt_pk_bf16_f32 v30, v30, v31\n"
    "s_branch .Lk_attn\n"
    ".Lk_q_retry:\n"
    "s_add_u32 s70, s70, 1\n"
    "s_cmp_gt_u32 s70, 0x200000\n"
    "s_cbranch_scc1 .Lk_q_ok\n"
    "s_sleep 1\n"
    "v_cmp_ne_u32 vcc, s68, v33\n"
    "v_cmp_ne_u32 s[94:95], s68, v35\n"
    "s_or_b64 vcc, vcc, s[94:95]\n"
    "s_mov_b64 exec, vcc\n"
    "s_cbranch_execz .Lk_u148\n"
    "buffer_load_dwordx4 v[32:35], v28, s[32:35], s83 offen offset:0 sc1\n"
    ".Lk_u148:\n"
    "s_mov_b64 exec, -1\n"
    "v_cmp_ne_u32 vcc, s68, v41\n"
    "s_cbranch_vccz .Lk_u149\n"
    "buffer_load_dwordx2 v[40:41], v19, s[32:35], s82 offen offset:0 sc1\n"
    ".Lk_u149:\n"
    "v_cmp_ne_u32 vcc, s68, v43\n"
    "s_cbranch_vccz .Lk_u150\n"
    "buffer_load_dwordx2 v[42:43], v19, s[32:35], s82 offen offset:512 sc1\n"
    ".Lk_u150:\n"
    "v_cmp_ne_u32 vcc, s68, v45\n"
    "s_cbranch_vccz .Lk_u151\n"
    "buffer_load_dwordx2 v[44:45], v19, s[32:35], s82 offen offset:1024 sc1\n"
    ".Lk_u151:\n"
    "v_cmp_ne_u32 vcc, s68, v47\n"
    "s_cbranch_vccz .Lk_u152\n"
    "buffer_load_dwordx2 v[46:47], v19, s[32:35], s82 offen offset:1536 sc1\n"
    ".Lk_u152:\n"
    "v_cmp_ne_u32 vcc, s68, v49\n"
    "s_cbranch_vccz .Lk_u153\n"
    "buffer_load_dwordx2 v[48:49], v19, s[32:35], s82 offen offset:2048 sc1\n"
    ".Lk_u153:\n"
    "v_cmp_ne_u32 vcc, s68, v51\n"
    "s_cbranch_vccz .Lk_u154\n"
    "buffer_load_dwordx2 v[50:51], v19, s[32:35], s82 offen offset:2560 sc1\n"
    ".Lk_u154:\n"
    "v_cmp_ne_u32 vcc, s68, v53\n"
    "s_cbranch_vccz .Lk_u155\n"
    "buffer_load_dwordx2 v[52:53], v19, s[32:35], s82 offen offset:3072 sc1\n"
    ".Lk_u155:\n"
    "s_waitcnt vmcnt(0)\n"
    "s_branch .Lk_q_use\n"
    ".Lk_r_retry:\n"
    "s_add_u32 s70, s70, 1\n"
    "s_cmp_gt_u32 s70, 0x200000\n"
    "s_cbranch_scc1 .Lk_r_ok\n"
    "s_sleep 1\n"
    "v_cmp_ne_u32 vcc, s68, v21\n"
    "s_cbranch_vccz .Lk_u156\n"
    "buffer_load_dwordx2 v[20:21], v19, s[32:35], s91 offen offset:0 sc1\n"
    ".Lk_u156:\n"
    "v_cmp_ne_u32 vcc, s68, v23\n"
    "s_cbranch_vccz .Lk_u157\n"
    "buffer_load_dwordx2 v[22:23], v19, s[32:35], s91 offen offset:512 sc1\n"
    ".Lk_u157:\n"
    "v_cmp_ne_u32 vcc, s68, v25\n"
    "s_cbranch_vccz .Lk_u158\n"
    "buffer_load_dwordx2 v[24:25], v19, s[32:35], s91 offen offset:1024 sc1\n"
    ".Lk_u158:\n"
    "v_cmp_ne_u32 vcc, s68, v27\n"
    "s_cbranch_vccz .Lk_u159\n"
    "buffer_load_dwordx2 v[26:27], v19, s[32:35], s91 offen offset:1536 sc1\n"
    ".Lk_u159:\n"
    "v_cmp_ne_u32 vcc, s68, v29\n"
    "s_cbranch_vccz .Lk_u160\n"
    "buffer_load_dwordx2 v[28:29], v19, s[32:35], s91 offen offset:2048 sc1\n"
    ".Lk_u160:\n"
    "v_cmp_ne_u32 vcc, s68, v31\n"
    "s_cbranch_vccz .Lk_u161\n"
    "buffer_load_dwordx2 v[30:31], v19, s[32:35], s91 offen offset:2560 sc1\n"
    ".Lk_u161:\n"
    "v_cmp_ne_u32 vcc, s68, v33\n"
    "s_cbranch_vccz .Lk_u162\n"
    "buffer_load_dwordx2 v[32:33], v19, s[32:35], s91 offen offset:3072 sc1\n"
    ".Lk_u162:\n"
    "s_waitcnt vmcnt(0)\n"
    "s_branch .Lk_r_use\n"
    ".Lk_a_retry:\n"
    "s_add_u32 s70, s70, 1\n"
    "s_cmp_gt_u32 s70, 0x200000\n"
    "s_cbranch_scc1 .Lk_a_ok\n"
    "v_cmp_ne_u32 vcc, s68, v37\n"
    "s_bcnt1_i32_b64 s71, vcc\n"
    "s_cmp_le_u32 s71, 8\n"
    "s_cbranch_scc1 .Lk_a_full\n"
    "s_sleep 8\n"
    "v_cmp_ne_u32 vcc, s68, v37\n"
    "s_mov_b64 exec, vcc\n"
    "s_cbranch_execz .Lk_u163\n"
    "v_and_b32 v45, 0x70, v1\n"
    "v_lshlrev_b32 v45, 3, v45\n"
    "buffer_load_dwordx4 v[36:39], v45, s[32:35], s87 offen offset:0 sc1\n"
    ".Lk_u163:\n"
    "s_mov_b64 exec, -1\n"
    "s_waitcnt vmcnt(0)\n"
    "s_branch .Lk_a_retry\n"
    ".Lk_a_full:\n"
    "s_sleep 1\n"
    "v_cmp_ne_u32 vcc, s68, v37\n"
    "v_cmp_ne_u32 s[94:95], s68, v39\n"
    "s_or_b64 vcc, vcc, s[94:95]\n"
    "s_mov_b64 exec, vcc\n"
    "s_cbranch_execz .Lk_u164\n"
    "v_and_b32 v45, 0x70, v1\n"
    "v_lshlrev_b32 v45, 3, v45\n"
    "buffer_load_dwordx4 v[36:39], v45, s[32:35], s87 offen offset:0 sc1\n"
    ".Lk_u164:\n"
    "s_mov_b64 exec, -1\n"
    "v_cmp_ne_u32 vcc, s68, v21\n"
    "s_cbranch_vccz .Lk_u165\n"
    "buffer_load_dwordx2 v[20:21], v19, s[32:35], s86 offen offset:0 sc1\n"
    ".Lk_u165:\n"
    "v_cmp_ne_u32 vcc, s68, v23\n"
    "s_cbranch_vccz .Lk_u166\n"
    "buffer_load_dwordx2 v[22:23], v19, s[32:35], s86 offen offset:512 sc1\n"
    ".Lk_u166:\n"
    "v_cmp_ne_u32 vcc, s68, v25\n"
    "s_cbranch_vccz .Lk_u167\n"
    "buffer_load_dwordx2 v[24:25], v19, s[32:35], s86 offen offset:1024 sc1\n"
    ".Lk_u167:\n"
    "v_cmp_ne_u32 vcc, s68, v27\n"
    "s_cbranch_vccz .Lk_u168\n"
    "buffer_load_dwordx2 v[26:27], v19, s[32:35], s86 offen offset:1536 sc1\n"
    ".Lk_u168:\n"
    "v_cmp_ne_u32 vcc, s68, v29\n"
    "s_cbranch_vccz .Lk_u169\n"
    "buffer_load_dwordx2 v[28:29], v19, s[32:35], s86 offen offset:2048 sc1\n"
    ".Lk_u169:\n"
    "v_cmp_ne_u32 vcc, s68, v31\n"
    "s_cbranch_vccz .Lk_u170\n"
    "buffer_load_dwordx2 v[30:31], v19, s[32:35], s86 offen offset:2560 sc1\n"
    ".Lk_u170:\n"
    "v_cmp_ne_u32 vcc, s68, v33\n"
    "s_cbranch_vccz .Lk_u171\n"
    "buffer_load_dwordx2 v[32:33], v19, s[32:35], s86 offen offset:3072 sc1\n"
    ".Lk_u171:\n"
    "v_cmp_ne_u32 vcc, s68, v35\n"
    "s_cbranch_vccz .Lk_u172\n"
    "buffer_load_dwordx2 v[34:35], v19, s[32:35], s86 offen offset:3584 sc1\n"
    ".Lk_u172:\n"
    "s_waitcnt vmcnt(0)\n"
    "s_branch .Lk_a_use\n"
    ".Lk_b_retry:\n"
    "s_add_u32 s70, s70, 1\n"
    "s_cmp_gt_u32 s70, 0x200000\n"
    "s_cbranch_scc1 .Lk_b_ok\n"
    "s_cmp_lg_u32 s69, 0\n"
    "s_cbranch_scc1 .Lk_b_full\n"
    "v_and_b32 v36, 0x70, v1\n"
    "v_lshlrev_b32 v36, 7, v36\n"
    "buffer_load_dwordx2 v[38:39], v36, s[32:35], s89 offen offset:0 sc1\n"
    "s_waitcnt vmcnt(0)\n"
    "v_cmp_ne_u32 vcc, s68, v39\n"
    "s_bcnt1_i32_b64 s71, vcc\n"
    "s_cmp_le_u32 s71, 8\n"
    "s_cbranch_scc1 .Lk_b_full2\n"
    "s_sleep 8\n"
    "s_branch .Lk_b_retry\n"
    ".Lk_b_full:\n"
    "s_sleep 1\n"
    ".Lk_b_full2:\n"
    "s_add_u32 s73, s89, 0x0\n"
    "v_cmp_ne_u32 vcc, s68, v21\n"
    "s_cbranch_vccz .Lk_u173\n"
    "buffer_load_dwordx2 v[20:21], v14, s[32:35], s73 offen offset:0 sc1\n"
    ".Lk_u173:\n"
    "s_add_u32 s73, s89, 0x800\n"
    "v_cmp_ne_u32 vcc, s68, v23\n"
    "s_cbranch_vccz .Lk_u174\n"
    "buffer_load_dwordx2 v[22:23], v14, s[32:35], s73 offen offset:0 sc1\n"
    ".Lk_u174:\n"
    "s_add_u32 s73, s89, 0x1000\n"
    "v_cmp_ne_u32 vcc, s68, v25\n"
    "s_cbranch_vccz .Lk_u175\n"
    "buffer_load_dwordx2 v[24:25], v14, s[32:35], s73 offen offset:0 sc1\n"
    ".Lk_u175:\n"
    "s_add_u32 s73, s89, 0x1800\n"
    "v_cmp_ne_u32 vcc, s68, v27\n"
    "s_cbranch_vccz .Lk_u176\n"
    "buffer_load_dwordx2 v[26:27], v14, s[32:35], s73 offen offset:0 sc1\n"
    ".Lk_u176:\n"
    "s_add_u32 s73, s89, 0x2000\n"
    "v_cmp_ne_u32 vcc, s68, v29\n"
    "s_cbranch_vccz .Lk_u177\n"
    "buffer_load_dwordx2 v[28:29], v14, s[32:35], s73 offen offset:0 sc1\n"
    ".Lk_u177:\n"
    "s_add_u32 s73, s89, 0x2800\n"
    "v_cmp_ne_u32 vcc, s68, v31\n"
    "s_cbranch_vccz .Lk_u178\n"
    "buffer_load_dwordx2 v[30:31], v14, s[32:35], s73 offen offset:0 sc1\n"
    ".Lk_u178:\n"
    "s_add_u32 s73, s89, 0x3000\n"
    "v_cmp_ne_u32 vcc, s68, v33\n"
    "s_cbranch_vccz .Lk_u179\n"
    "buffer_load_dwordx2 v[32:33], v14, s[32:35], s73 offen offset:0 sc1\n"
    ".Lk_u179:\n"
    "s_add_u32 s73, s89, 0x3800\n"
    "v_cmp_ne_u32 vcc, s68, v35\n"
    "s_cbranch_vccz .Lk_u180\n"
    "buffer_load_dwordx2 v[34:35], v14, s[32:35], s73 offen offset:0 sc1\n"
    ".Lk_u180:\n"
    "s_waitcnt vmcnt(0)\n"
    "v_xor_b32 v36, s68, v21\n"
    "v_bitop3_b32 v36, v23, v36, s68 bitop3:0xde\n"
    "v_bitop3_b32 v36, v25, v36, s68 bitop3:0xde\n"
    "v_bitop3_b32 v36, v27, v36, s68 bitop3:0xde\n"
    "v_bitop3_b32 v36, v29, v36, s68 bitop3:0xde\n"
    "v_bitop3_b32 v36, v31, v36, s68 bitop3:0xde\n"
    "v_bitop3_b32 v36, v33, v36, s68 bitop3:0xde\n"
    "v_bitop3_b32 v36, v35, v36, s68 bitop3:0xde\n"
    "v_cmp_ne_u32 vcc, 0, v36\n"
    "s_cbranch_vccz .Lk_b_use\n"
    "s_add_u32 s70, s70, 1\n"
    "s_cmp_gt_u32 s70, 0x200000\n"
    "s_cbranch_scc1 .Lk_b_use\n"
    "s_branch .Lk_b_full\n"
    ".Lk_s7:\n"
    "s_cmp_lg_u32 s69, 0\n"
    "s_cbranch_scc1 .Lk_s7end\n"
    "v_lshlrev_b32 v20, 3, v1\n"
    "buffer_load_dword v56, v20, s[44:47], 0 offen offset:0\n"
    "s_mov_b32 s72, 0x1000\n"
    "buffer_load_dword v56, v20, s[44:47], s72 offen offset:384\n"
    "v_and_b32 v25, 0xf80, v20\n"
    "v_lshrrev_b32 v26, 9, v1\n"
    "v_mul_u32_u24 v26, 0x28000, v26\n"
    "v_add_u32 v25, v25, v26\n"
    "s_add_u32 s73, s82, 0x91000\n"
    "buffer_load_dword v56, v25, s[32:35], s73 offen\n"
    "v_and_b32 v25, 0x380, v20\n"
    "v_bfe_u32 v26, v1, 7, 1\n"
    "v_mul_u32_u24 v26, 0x28000, v26\n"
    "v_add_u32 v24, v25, v26\n"
    "s_add_u32 s74, s83, 0x91000\n"
    "buffer_load_dword v56, v24, s[32:35], s74 offen\n"
    "v_and_b32 v25, 0x780, v20\n"
    "v_min_u32 v25, 0x680, v25\n"
    "v_bfe_u32 v26, v1, 8, 1\n"
    "v_lshl_add_u32 v27, v26, 14, v25\n"
    "s_add_u32 s73, s88, 0x90900\n"
    "buffer_load_dword v56, v27, s[32:35], s73 offen\n"
    "v_mul_u32_u24 v26, 0x3800, v26\n"
    "v_add_u32 v25, v25, v26\n"
    "s_mul_i32 s74, s64, 0x7000\n"
    "s_mul_i32 s73, s65, 1792\n"
    "s_add_u32 s74, s74, s73\n"
    "s_add_u32 s74, s74, 0xe1000\n"
    "buffer_load_dword v56, v25, s[32:35], s74 offen\n"
    "s_mul_i32 s73, s65, 1792\n"
    "s_add_u32 s73, s73, 0x50000\n"
    "v_mov_b32 v20, 0\n"
    ".Lk_s7poll:\n"
    "s_mov_b64 exec, 1\n"
    "buffer_load_dwordx2 v[22:23], v20, s[32:35], s73 offen offset:0 sc1\n"
    "s_waitcnt vmcnt(0)\n"
    "v_cmp_ne_u32 vcc, s68, v23\n"
    "s_mov_b64 exec, -1\n"
    "s_cbranch_vccz .Lk_s7go\n"
    "s_add_u32 s70, s70, 1\n"
    "s_cmp_gt_u32 s70, 0x200000\n"
    "s_cbranch_scc1 .Lk_s7go\n"
    "s_sleep 2\n"
    "s_branch .Lk_s7poll\n"
    ".Lk_s7go:\n"
    "s_add_u32 s32, s32, 0x91000\n"
    "s_addc_u32 s33, s33, 0\n"
    "s_mov_b32 s34, 0x16f000\n"
    "s_sleep 60\n"
    "s_branch .Lk_q_go\n"
    ".Lk_final:\n"
    "s_cmp_lg_u32 s64, 0\n"
    "s_cbranch_scc1 .Lk_end\n"
    "s_mul_i32 s71, s67, 896\n"
    "s_lshl_b32 s72, s66, 7\n"
    "s_add_u32 s71, s71, s72\n"
    "s_mov_b64 s[56:57], s[20:21]\n"
    "s_add_u32 s56, s56, s71\n"
    "s_addc_u32 s57, s57, 0\n"
    "s_sub_u32 s58, 0xe00, s71\n"
    "s_and_b32 s57, s57, 0xffff\n"
    "s_mov_b32 s59, 0x20000\n"
    "v_lshrrev_b32 v20, 2, v19\n"
    "buffer_store_short v28, v20, s[56:59], 0 offen\n"
    "s_branch .Lk_end\n"
    ".Lk_s7end:\n"
    "s_cmp_lg_u32 s64, 0\n"
    "s_cbranch_scc1 .Lk_end\n"
    "s_cmp_lg_u32 s65, 0\n"
    "s_cbranch_scc1 .Lk_end\n"
    "s_mov_b32 s71, 0x90000\n"
    "v_mov_b32 v21, s68\n"
    "v_mov_b32 v22, 0\n"
    "s_mov_b64 exec, 1\n"
    "buffer_store_dword v21, v22, s[32:35], s71 offen sc1\n"
    "s_mov_b64 exec, -1\n"
    ".Lk_end:\n"
      : "+v"(tid)
      : "s"(__builtin_amdgcn_kernarg_segment_ptr()), "s"(blk), "s"((unsigned)(size_t)lds)
      :
      "v1", "v2", "v3", "v4", "v5", "v6", "v7", "v8", "v9", "v10", "v11", "v12", "v13", "v14", "v15", "v16",
      "v17", "v18", "v19", "v20", "v21", "v22", "v23", "v24", "v25", "v26", "v27", "v28", "v29", "v30", "v31", "v32",
      "v33", "v34", "v35", "v36", "v37", "v38", "v39", "v40", "v41", "v42", "v43", "v44", "v45", "v46", "v47", "v48",
      "v49", "v50", "v51", "v52", "v53", "v54", "v55", "v56", "v57", "v58", "v59", "v60", "v61", "v62", "v63", "v64",
      "v65", "v66", "v67", "v68", "v69", "v70", "v71", "v72", "v73", "v74", "v75", "v76", "v77", "v78", "v79", "v80",
      "v81", "v82", "v83", "v84", "v85", "v86", "v87", "v88", "v89", "v90", "v91", "v92", "v93", "v94", "v95", "v96",
      "v97", "v98", "v99", "v100", "v101", "v102", "v103", "v104", "v105", "v106", "v107", "v108", "v109", "v110", "v111", "v112",
      "v113", "v114", "v115", "v116", "v117", "v118", "v119", "v120", "v121", "v122", "v123", "v124", "v125", "v126", "v127", "v128",
      "v129", "v130", "v131", "v132", "v133", "v134", "v135", "v136", "v137", "v138", "v139", "v140", "v141", "v142", "v143", "v144",
      "v145", "v146", "v147", "v148", "v149", "v150", "v151", "v152", "v153", "v154", "v155", "v156", "v157", "v158", "v159", "v160",
      "v161", "v162", "v163", "v164", "v165", "v166", "v167", "v168", "v169", "v170", "v171", "v172", "v173", "v174", "v175", "v176",
      "v177", "v178", "v179", "v180", "v181", "v182", "v183", "v184", "v185", "v186", "v187", "v188", "v189", "v190", "v191", "v192",
      "v193", "v194", "v195", "v196", "v197", "v198", "v199", "v200", "v201", "v202", "v203", "v204", "v205", "v206", "v207", "v208",
      "v209", "v210", "v211", "v212", "v213", "v214", "v215", "v216", "v217", "v218", "v219", "v220", "v221", "v222", "v223", "v224",
      "v225", "v226", "v227", "v228", "v229", "v230", "v231", "v232", "v233", "v234", "v235", "v236", "v237", "v238", "v239", "v240",
      "v241", "v242", "v243", "v244", "v245", "v246", "v247", "v248", "v249", "v250", "v251", "v252", "v253", "v254", "v255", "a0",
      "a1", "a2", "a3", "a4", "a5", "a6", "a7", "a8", "a9", "a10", "a11", "a12", "a13", "a14", "a15", "a16",
      "a17", "a18", "a19", "a20", "a21", "a22", "a23", "a24", "a25", "a26", "a27", "a28", "a29", "a30", "a31", "a32",
      "a33", "a34", "a35", "a36", "a37", "a38", "a39", "a40", "a41", "a42", "a43", "a44", "a45", "a46", "a47", "a48",
      "a49", "a50", "a51", "a52", "a53", "a54", "a55", "a56", "a57", "a58", "a59", "a60", "a61", "a62", "a63", "a64",
      "a65", "a66", "a67", "a68", "a69", "a70", "a71", "a72", "a73", "a74", "a75", "a76", "a77", "a78", "a79", "a80",
      "a81", "a82", "a83", "a84", "a85", "a86", "a87", "a88", "a89", "a90", "a91", "a92", "a93", "a94", "a95", "a96",
      "a97", "a98", "a99", "a100", "a101", "a102", "a103", "a104", "a105", "a106", "a107", "a108", "a109", "a110", "a111", "a112",
      "a113", "a114", "a115", "a116", "a117", "a118", "a119", "a120", "a121", "a122", "a123", "a124", "a125", "a126", "a127", "a128",
      "a129", "a130", "a131", "a132", "a133", "a134", "a135", "a136", "a137", "a138", "a139", "a140", "a141", "a142", "a143", "a144",
      "a145", "a146", "a147", "a148", "a149", "a150", "a151", "a152", "a153", "a154", "a155", "a156", "a157", "a158", "a159", "a160",
      "a161", "a162", "a163", "a164", "a165", "a166", "a167", "a168", "a169", "a170", "a171", "a172", "a173", "a174", "a175", "a176",
      "a177", "a178", "a179", "a180", "a181", "a182", "a183", "a184", "a185", "a186", "a187", "a188", "a189", "a190", "a191", "a192",
      "a193", "a194", "a195", "a196", "a197", "a198", "a199", "a200", "a201", "a202", "a203", "a204", "a205", "a206", "a207", "a208",
      "a209", "a210", "a211", "a212", "a213", "a214", "a215", "a216", "a217", "a218", "a219", "a220", "a221", "a222", "a223", "a224",
      "a225", "a226", "a227", "a228", "a229", "a230", "a231", "a232", "a233", "a234", "a235", "a236", "a237", "a238", "a239", "a240",
      "a241", "a242", "a243", "a244", "a245", "a246", "a247", "a248", "a249", "a250", "a251", "a252", "a253", "a254", "a255", "s4",
      "s5", "s6", "s7", "s8", "s9", "s10", "s11", "s12", "s13", "s14", "s15", "s16", "s17", "s18", "s19", "s20",
      "s21", "s22", "s23", "s24", "s25", "s26", "s27", "s28", "s29", "s30", "s31", "s32", "s33", "s34", "s35", "s36",
      "s37", "s38", "s39", "s40", "s41", "s42", "s43", "s44", "s45", "s46", "s47", "s48", "s49", "s50", "s51", "s52",
      "s53", "s54", "s55", "s56", "s57", "s58", "s59", "s60", "s61", "s62", "s63", "s64", "s65", "s66", "s67", "s68",
      "s69", "s70", "s71", "s72", "s73", "s74", "s75", "s76", "s77", "s78", "s79", "s80", "s81", "s82", "s83", "s84",
      "s85", "s86", "s87", "s88", "s89", "s90", "s91", "s92", "s93", "s94", "s95", "s96", "s97", "s98", "s99", "s100",
      "s101", "vcc", "m0", "scc", "memory"
  );
}

extern "C" void kernel_launch(void* const* d_in, const int* in_sizes, int n_in, void* d_out, int out_size,
                              void* d_ws, size_t ws_size, hipStream_t stream) {
  (void)in_sizes; (void)n_in; (void)out_size; (void)ws_size;
  fused_decode_kernel<<<256, 256, 0, stream>>>(d_in[0], d_in[1], d_in[2], d_in[3], d_in[4], d_in[6], d_in[7],
                                               d_in[8], d_out, d_ws);
}
```
